```python
import torch

def _softmax(x, axis=-1):
    return torch.softmax(x, dim=axis)
def _gelu(x):
    return torch.nn.functional.gelu(x, approximate="tanh")
def _silu(x):
    return torch.nn.functional.silu(x)
def _sigmoid(x):
    return torch.sigmoid(x)
def _layernorm(x, axis=-1, eps=1e-5):
    m = x.mean(dim=axis, keepdim=True)
    v = x.var(dim=axis, keepdim=True, unbiased=False)
    return (x - m) / torch.sqrt(v + eps)
def _scatter_add(tgt, idx, src, axis=0):
    return tgt.clone().index_add_(axis, idx, src)
def _segment_sum(src, seg, n):
    out = torch.zeros((n,) + src.shape[1:], dtype=src.dtype)
    return out.index_add_(0, seg, src)
def _pad(x, pw):
    flat = tuple(v for pair in reversed(pw) for v in pair)
    return torch.nn.functional.pad(x, flat)

kernel_name = 'recurrent_scan__sliding_block__geglu_ffn__prompt'

def setup_inputs():

    _w = (lambda a: (lambda t: t.to(torch.bfloat16) if t.is_floating_point() else t)(torch.as_tensor(a)))
    def _A_setup():
        B, S, D = 1, 2048, 512
        return {
            "x": _w((lambda *s: torch.randn(*s, dtype=torch.bfloat16) * 0.05)(B, S, D)),
            "wqkv": _w((lambda *s: torch.randn(*s, dtype=torch.bfloat16) * 0.05)(D, 3 * D)),
            "wo": _w((lambda *s: torch.randn(*s, dtype=torch.bfloat16) * 0.05)(D, D)),
        }
    _A_in = _A_setup()

    def _B_setup():
        B, S, D, F = 1, 2048, 512, 1792
        return {
            "x": _w((lambda *s: torch.randn(*s, dtype=torch.bfloat16) * 0.05)(B, S, D)),
            "w_gate": _w((lambda *s: torch.randn(*s, dtype=torch.bfloat16) * 0.05)(D, F)),
            "w_up": _w((lambda *s: torch.randn(*s, dtype=torch.bfloat16) * 0.05)(D, F)),
            "w_down": _w((lambda *s: torch.randn(*s, dtype=torch.bfloat16) * 0.05)(F, D)),
        }
    _B_in = _B_setup()

    return {
        "A_x": _A_in["x"],
        "A_wqkv": _A_in["wqkv"],
        "A_wo": _A_in["wo"],
        "B_x": _B_in["x"],
        "B_w_gate": _B_in["w_gate"],
        "B_w_up": _B_in["w_up"],
        "B_w_down": _B_in["w_down"],
    }

def reference(A_x, A_wqkv, A_wo, B_x, B_w_gate, B_w_up, B_w_down):
    def _A_ref(x, wqkv, wo):
        B, S, D = x.shape
        H = 32; Dh = D // H; W = 256
        qkv = (lambda x, *d: x.permute(*d))((x @ wqkv).reshape(B, S, 3, H, Dh), 2, 0, 3, 1, 4)
        q, k, v = qkv[0], qkv[1], qkv[2]
        scores = torch.einsum("bhqd,bhkd->bhqk", q, k) * (Dh ** -0.5)
        idx = torch.arange(S)
        mask = (idx[:, None] - idx[None, :] >= 0) & (idx[:, None] - idx[None, :] < W)
        scores = torch.where(mask[None, None], scores, -1e9)
        p = _softmax(scores, axis=-1)
        o = torch.einsum("bhqk,bhkd->bhqd", p, v)
        return (lambda x, *d: x.permute(*d))(o, 0, 2, 1, 3).reshape(B, S, D) @ wo

    def _B_ref(x, w_gate, w_up, w_down):
        return (_gelu(x @ w_gate) * (x @ w_up)) @ w_down

    x = A_x
    xn = _layernorm(x, axis=-1)
    s = _A_ref(xn, A_wqkv, A_wo)
    g = _silu(_B_ref(xn, B_w_gate, B_w_up, B_w_down))
    return x + s * g
```

```cpp
#include <hip/hip_runtime.h>
#include <hip/hip_bf16.h>
typedef unsigned short u16;
__device__ __forceinline__ float bf2f(u16 b){ return __uint_as_float(((unsigned)b)<<16); }
__device__ __forceinline__ u16 f2bf(float f){ unsigned u=__float_as_uint(f); u += 0x7fffu + ((u>>16)&1u); return (u16)(u>>16); }
__device__ __forceinline__ float rbf(float f){ return bf2f(f2bf(f)); }
constexpr int S=2048, D=512, F=1792, H=32, DH=16, W=256;

__global__ void k_ln(const u16* x, u16* xn){
  int row=blockIdx.x; int t=threadIdx.x;
  float v[8]; float s=0;
  for(int i=0;i<8;i++){ v[i]=bf2f(x[row*D+t*8+i]); s+=v[i]; }
  for(int m=32;m>=1;m>>=1) s+=__shfl_xor(s,m);
  float mean=s/D; float q=0;
  for(int i=0;i<8;i++){ float d=v[i]-mean; q+=d*d; }
  for(int m=32;m>=1;m>>=1) q+=__shfl_xor(q,m);
  float var=rbf(q/D); float mb=rbf(mean);
  float den=rbf(sqrtf(rbf(var+1e-5f)));
  for(int i=0;i<8;i++){ xn[row*D+t*8+i]=f2bf(rbf(v[i]-mb)/den); }
}
__global__ void k_gemm(const u16* A, const u16* B, u16* C, int M, int N, int K){
  int n=blockIdx.x*64+threadIdx.x; int m=blockIdx.y;
  float acc=0;
  for(int k=0;k<K;k++) acc+=bf2f(A[m*K+k])*bf2f(B[k*N+n]);
  C[m*N+n]=f2bf(acc);
}
__global__ void k_h(const u16* g, const u16* u, u16* h, int n){
  int i=blockIdx.x*256+threadIdx.x; if(i>=n) return;
  float x=bf2f(g[i]);
  float t=tanhf(0.7978845608028654f*(x+0.044715f*x*x*x));
  float ge=rbf(0.5f*x*(1.f+t));
  h[i]=f2bf(ge*bf2f(u[i]));
}
__global__ void k_attn(const u16* qkv, u16* o){
  int q=blockIdx.x; int h=blockIdx.y; int t=threadIdx.x;
  __shared__ float p[256]; __shared__ float red[256];
  int j=q-t; float s=-1e30f;
  if(j>=0){ float a=0; for(int d=0;d<DH;d++) a+=bf2f(qkv[q*1536+h*DH+d])*bf2f(qkv[j*1536+512+h*DH+d]); s=rbf(a)*0.25f; }
  red[t]=s; __syncthreads();
  for(int m=128;m>=1;m>>=1){ if(t<m) red[t]=fmaxf(red[t],red[t+m]); __syncthreads(); }
  float mx=red[0]; __syncthreads();
  float e=(j>=0)?expf(s-mx):0.f; red[t]=e; __syncthreads();
  for(int m=128;m>=1;m>>=1){ if(t<m) red[t]+=red[t+m]; __syncthreads(); }
  float sum=red[0];
  p[t]=rbf(e/sum); __syncthreads();
  if(t<DH){ float a=0; for(int i=0;i<256;i++){ int jj=q-i; if(jj>=0) a+=p[i]*bf2f(qkv[jj*1536+1024+h*DH+t]); } o[q*D+h*DH+t]=f2bf(a); }
}
__global__ void k_final(const u16* x, const u16* s, const u16* g, u16* out, int n){
  int i=blockIdx.x*256+threadIdx.x; if(i>=n) return;
  float gg=bf2f(g[i]); float sl=rbf(gg/(1.f+expf(-gg)));
  out[i]=f2bf(bf2f(x[i])+rbf(bf2f(s[i])*sl));
}
extern "C" void kernel_launch(void* const* d_in, const int* in_sizes, int n_in, void* d_out, int out_size, void* d_ws, size_t ws_size, hipStream_t stream){
  const u16* x=(const u16*)d_in[0]; const u16* wqkv=(const u16*)d_in[1]; const u16* wo=(const u16*)d_in[2];
  const u16* wg=(const u16*)d_in[4]; const u16* wu=(const u16*)d_in[5]; const u16* wd=(const u16*)d_in[6];
  u16* ws=(u16*)d_ws;
  u16* xn=ws; u16* qkv=xn+S*D; u16* g=qkv+S*1536; u16* u=g+S*F; u16* h=u+S*F; u16* o=h+S*F; u16* s=o+S*D; u16* gg=s+S*D;
  k_ln<<<S,64,0,stream>>>(x,xn);
  k_gemm<<<dim3(1536/64,S),64,0,stream>>>(xn,wqkv,qkv,S,1536,D);
  k_gemm<<<dim3(F/64,S),64,0,stream>>>(xn,wg,g,S,F,D);
  k_gemm<<<dim3(F/64,S),64,0,stream>>>(xn,wu,u,S,F,D);
  k_h<<<(S*F+255)/256,256,0,stream>>>(g,u,h,S*F);
  k_attn<<<dim3(S,H),256,0,stream>>>(qkv,o);
  k_gemm<<<dim3(D/64,S),64,0,stream>>>(o,wo,s,S,D,D);
  k_gemm<<<dim3(D/64,S),64,0,stream>>>(h,wd,gg,S,D,F);
  k_final<<<(S*D+255)/256,256,0,stream>>>(x,s,gg,(u16*)d_out,S*D);
}
```

```cpp
#include <hip/hip_runtime.h>
#include <hip/hip_bf16.h>
#include <utility>

typedef unsigned v4u __attribute__((ext_vector_type(4)));
typedef unsigned v2u __attribute__((ext_vector_type(2)));
typedef float f32x2 __attribute__((ext_vector_type(2)));
typedef float f32x4 __attribute__((ext_vector_type(4)));
typedef float f32x16 __attribute__((ext_vector_type(16)));
typedef __bf16 bf16x2 __attribute__((ext_vector_type(2)));
typedef __bf16 bf16x8 __attribute__((ext_vector_type(8)));
typedef short s16x4 __attribute__((ext_vector_type(4)));
typedef unsigned long long u64;
#define LAS __attribute__((address_space(3)))
#define GAS __attribute__((address_space(1)))
#define DI __device__ __forceinline__

#define ROT 1

constexpr u64 WS_EPOCH = 0;
constexpr u64 WS_FLAG = 4096;
constexpr u64 WS_QKV = 1u << 20;
constexpr u64 WS_H = WS_QKV + (6u << 20);
constexpr u64 WS_O = WS_H + (7u << 20);
constexpr u64 WS_STAMP = 32u << 20;
constexpr int SLOT = 8192;
constexpr int MISC = 19 * SLOT;
constexpr int L_SP = MISC;
constexpr int L_ST = MISC + 3072;
constexpr int L_CNT = MISC + 5120;
constexpr int L_SCR = MISC + 6144;
constexpr int L_K = 1 * SLOT;
constexpr int L_V = 5 * SLOT;
constexpr int LDS_BYTES = 20 * SLOT;

#define MFMA(a, b, c) __builtin_amdgcn_mfma_f32_32x32x16_bf16((a), (b), (c), 0, 0, 0)
#define SCHED() __builtin_amdgcn_sched_barrier(0)

template <int LIMM> DI void glds(unsigned voff, u64 sbase, unsigned soff, unsigned lbase) {
  asm volatile("s_add_u32 m0, %4, %5\n\ts_add_u32 s90, %1, %3\n\ts_addc_u32 s91, %2, 0\n\tglobal_load_lds_dwordx4 %0, s[90:91]"
               ::"v"(voff), "s"((unsigned)sbase), "s"((unsigned)(sbase >> 32)), "s"(soff), "s"(lbase), "n"(LIMM)
               : "memory", "scc", "s90", "s91");
}
template <int LIMM, int SIMM> DI void gldsi(unsigned voff, u64 sbase, unsigned lbase) {
  asm volatile("s_add_u32 m0, %3, %4\n\ts_add_u32 s90, %1, %5\n\ts_addc_u32 s91, %2, 0\n\tglobal_load_lds_dwordx4 %0, s[90:91]"
               ::"v"(voff), "s"((unsigned)sbase), "s"((unsigned)(sbase >> 32)), "s"(lbase), "n"(LIMM), "n"(SIMM)
               : "memory", "scc", "s90", "s91");
}
#define OPQ_S(x) asm volatile("" : "+s"(x))
template <int N> DI void waitbar() { asm volatile("s_waitcnt vmcnt(%0)\n\ts_barrier" ::"n"(N) : "memory"); }
template <int N> DI void waitbar_l() { asm volatile("s_waitcnt vmcnt(%0) lgkmcnt(0)\n\ts_barrier" ::"n"(N) : "memory"); }
DI void bar_l() { asm volatile("s_waitcnt lgkmcnt(0)\n\ts_barrier" ::: "memory"); }
template <int N> DI void waitv() { asm volatile("s_waitcnt vmcnt(%0)" ::"n"(N) : "memory"); }
template <int OFF> DI void ld16(v4u& d, unsigned voff, u64 sbase) {
  asm volatile("global_load_dwordx4 %0, %1, %2 offset:%3 sc1" : "=v"(d) : "v"(voff), "s"(sbase), "n"(OFF) : "memory");
}
template <int OFF> DI void st16(unsigned voff, v4u d, u64 sbase) {
  asm volatile("global_store_dwordx4 %0, %1, %2 offset:%3 sc1\n\ts_nop 1" ::"v"(voff), "v"(d), "s"(sbase), "n"(OFF) : "memory");
}
DI void ld8(v2u& d, unsigned voff, u64 sbase) {
  asm volatile("global_load_dwordx2 %0, %1, %2 sc1" : "=v"(d) : "v"(voff), "s"(sbase) : "memory");
}
#define TOUCH(x) asm volatile("" : "+v"(x))

DI bf16x8 trfrag(unsigned a0, unsigned a1) {
  const s16x4 lo = __builtin_amdgcn_ds_read_tr16_b64_v4i16((LAS s16x4*)(size_t)a0);
  const s16x4 hi = __builtin_amdgcn_ds_read_tr16_b64_v4i16((LAS s16x4*)(size_t)a1);
  return __builtin_bit_cast(bf16x8, __builtin_shufflevector(lo, hi, 0, 1, 2, 3, 4, 5, 6, 7));
}
DI bf16x8 ldsfrag(unsigned a) { return *(LAS bf16x8*)(size_t)a; }
DI unsigned pk2(float a, float b) { return __builtin_bit_cast(unsigned, __builtin_convertvector((f32x2){a, b}, bf16x2)); }
DI float bfr(float a) { return __builtin_bit_cast(float, pk2(a, a) & 0xffff0000u); }
DI float dot1(bf16x8 f, float acc) {
  const v4u u = __builtin_bit_cast(v4u, f);
  const bf16x2 one = {(__bf16)1.0f, (__bf16)1.0f};
#pragma unroll
  for (int i = 0; i < 4; ++i) { const unsigned e = u[i]; acc = __builtin_amdgcn_fdot2_f32_bf16(__builtin_bit_cast(bf16x2, e), one, acc, false); }
  return acc;
}
DI float dotsq(bf16x8 f, float acc) {
  const v4u u = __builtin_bit_cast(v4u, f);
#pragma unroll
  for (int i = 0; i < 4; ++i) { const unsigned e = u[i]; acc = __builtin_amdgcn_fdot2_f32_bf16(__builtin_bit_cast(bf16x2, e), __builtin_bit_cast(bf16x2, e), acc, false); }
  return acc;
}
DI float halfsum(float v) {
  const unsigned u = __builtin_bit_cast(unsigned, v);
  const auto r = __builtin_amdgcn_permlane32_swap(u, u, false, false);
  const unsigned r0 = r[0], r1 = r[1];
  return __builtin_bit_cast(float, r0) + __builtin_bit_cast(float, r1);
}
DI float halfmax(float v) {
  const unsigned u = __builtin_bit_cast(unsigned, v);
  const auto r = __builtin_amdgcn_permlane32_swap(u, u, false, false);
  const unsigned r0 = r[0], r1 = r[1];
  return fmaxf(__builtin_bit_cast(float, r0), __builtin_bit_cast(float, r1));
}
DI v4u pack8(const f32x16& a, int o, float s) {
  return (v4u){pk2(a[o] * s, a[o + 1] * s), pk2(a[o + 2] * s, a[o + 3] * s), pk2(a[o + 4] * s, a[o + 5] * s), pk2(a[o + 6] * s, a[o + 7] * s)};
}
DI float geglu(float ga, float ua, float rs) {
  const float g = ga * rs;
  const float e = __builtin_amdgcn_exp2f(g * __builtin_fmaf(g * g, -0.10294324f, -2.3022082f));
  return (g * ua) * (__builtin_amdgcn_rcpf(1.0f + e) * rs);
}
DI v4u geglu8(const f32x16& g, const f32x16& u, int o, float rs) {
  float h[8];
#pragma unroll
  for (int i = 0; i < 8; ++i) h[i] = geglu(g[o + i], u[o + i], rs);
  return (v4u){pk2(h[0], h[1]), pk2(h[2], h[3]), pk2(h[4], h[5]), pk2(h[6], h[7])};
}
DI unsigned hilo(float s) {
  const float hi = bfr(s);
  return pk2(hi, s - hi);
}
DI float max16(const f32x16& s, float m) {
#pragma unroll
  for (int i = 0; i < 16; i += 2) m = __builtin_fmaxf(__builtin_fmaxf(m, s[i]), s[i + 1]);
  return m;
}
DI void poll_finish(v2u v, bool act, unsigned voff, u64 sbase, unsigned t0, unsigned t1) {
#pragma unroll 1
  for (unsigned spins = 0; spins < (1u << 14); ++spins) {
    const bool ok = !act || (v.x == t0 && v.y == t1);
    if (__builtin_amdgcn_ballot_w64(ok) == ~0ull) break;
    __builtin_amdgcn_s_sleep(2);
    asm volatile("global_load_dwordx2 %0, %1, %2 sc1\n\ts_waitcnt vmcnt(0)" : "=v"(v) : "v"(voff), "s"(sbase) : "memory");
  }
}
template <int I> using IC = std::integral_constant<int, I>;
template <class F, int... Is> DI void sfor_impl(F&& f, std::integer_sequence<int, Is...>) { (f(IC<Is>{}), ...); }
template <int N, class F> DI void sfor(F&& f) { sfor_impl(f, std::make_integer_sequence<int, N>{}); }
constexpr int TS[8] = {15, 17, 10, 12, 0, 2, 5, 7};
constexpr int CS[9][4] = {{14, 4, 9, 15}, {16, 17, 18, 10}, {11, 12, 13, 0}, {5, 6, 7, 8}, {1, 2, 3, 14}, {4, 9, 15, 16}, {17, 18, 10, 11}, {12, 13, 0, 5}, {6, 7, 8, 1}};
DI int opq_tid() { int t = threadIdx.x; asm volatile("" : "+v"(t)); return t; }
DI void put_flag(u64 a, unsigned t0, unsigned t1) {
  __hip_atomic_store((GAS u64*)a, (u64)t0 | ((u64)t1 << 32), __ATOMIC_RELAXED, __HIP_MEMORY_SCOPE_AGENT);
}
#define STAMP(i)

__global__ void __launch_bounds__(512, 2)
fused_block(const char* __restrict__ xg, const char* __restrict__ wqkv, const char* __restrict__ wo, const char* __restrict__ wg,
            const char* __restrict__ wu, const char* __restrict__ wd, char* __restrict__ outg, char* __restrict__ wsp) {
  __shared__ __attribute__((aligned(1024))) char lds[LDS_BYTES];
  u64 px = (u64)xg, pq = (u64)wqkv, po = (u64)wo, pg = (u64)wg, pu = (u64)wu, pd = (u64)wd, pout = (u64)outg, ws = (u64)wsp;
  asm volatile("" : "+s"(px), "+s"(pq), "+s"(po), "+s"(pg), "+s"(pu), "+s"(pd), "+s"(pout), "+s"(ws));
  const int w = __builtin_amdgcn_readfirstlane(threadIdx.x >> 6);
  const int b = blockIdx.x, xcd = b & 7, idx = b >> 3;
  const int j = 4 * (xcd & 3) + (idx & 3), r = 8 * (xcd >> 2) + (idx >> 2);
  const int rt = 4 * xcd + (idx & 3), ct = idx >> 2;
  const bool typeC = j >= 12;
  const unsigned L0 = (unsigned)(size_t)(LAS char*)lds;
  const unsigned w1k = L0 + w * 1024;
  const int tt = w & 3, wc = w >> 2;
  const unsigned l16 = (threadIdx.x & 63) * 16;
  STAMP(0);

  if (w == 0) {
    u64 pc;
    unsigned len;
    asm volatile("s_getpc_b64 %0\n.Lfb_here:\n\ts_mov_b32 %1, .Lfb_end-.Lfb_here" : "=s"(pc), "=s"(len));
    if (idx * 1024 + 1024 <= len) glds<L_SCR>(l16, pc & ~(u64)127, idx * 1024, L0);
  }
  v2u ep;
  ld8(ep, 0u, ws + WS_EPOCH);
  const int rot = ROT ? ((r + 2 * (idx & 3)) & 7) : 0;
  const int unit0 = typeC ? 3 * j - 24 : j;
  unsigned vox, voq, vog, voc, vo12;
  {
    const int lane = threadIdx.x & 63;
    const unsigned rowi = 8 * w + (lane >> 3);
    const unsigned wsw = ((lane & 7) ^ (((lane >> 4) & 1) << 2)) * 16;
    vox = rowi * 1024 + (((lane & 7) ^ ((4 * (w & 1) + (lane >> 4)) & 7)) * 16);
    voq = rowi * 3072 + wsw; vog = rowi * 3584 + wsw; voc = rowi * 1024 + wsw;
    vo12 = typeC ? voq : vog;
  }
  const u64 bx = px + (u64)r * 131072;
  const u64 bu0 = pq + unit0 * 128;
  const u64 bu1 = typeC ? bu0 + 128 : pg + j * 128;
  const u64 bu2 = typeC ? bu0 + 256 : pu + j * 128;
  const unsigned cs12 = typeC ? 64 * 3072 : 64 * 3584;
  const u64 bwd = pd + ct * 128, bwo = po + ct * 128;

  auto fillF = [&](auto S) {
    constexpr int s = S, sl = (s % 3) * 5 * SLOT;
    unsigned ro = rot; OPQ_S(ro);
    const unsigned kc = (s + ro) & 7;
    glds<sl>(vox, bx, kc * 128, w1k);
    glds<sl + SLOT>(vox, bx, kc * 128 + 65536, w1k);
    glds<sl + 2 * SLOT>(voq, bu0, kc * (64 * 3072), w1k);
    glds<sl + 3 * SLOT>(vo12, bu1, kc * cs12, w1k);
    glds<sl + 4 * SLOT>(vo12, bu2, kc * cs12, w1k);
  };
  auto fillT = [&](auto T) {
    constexpr int t = T;
    unsigned ro = rot; OPQ_S(ro);
    const unsigned o = ((t + ro) & 7) * (64 * 3584) + (12 + j) * 128;
    glds<TS[t] * SLOT>(vog, pg, o, w1k);
    glds<(TS[t] + 1) * SLOT>(vog, pu, o, w1k);
  };
  auto fillC = [&](auto ST, auto Q) {
    constexpr int st = ST, q = Q;
    if constexpr (st < 3) gldsi<CS[st][q] * SLOT, (3 * q + st) * 65536>(voc, bwd, w1k);
    else if constexpr (st < 7) gldsi<CS[st][q] * SLOT, (12 + 4 * q + st - 3) * 65536>(voc, bwd, w1k);
    else gldsi<CS[st][q] * SLOT, (2 * q + st - 7) * 65536>(voc, bwo, w1k);
  };
#define FF(s) fillF(IC<s>{})
#define FT(t) fillT(IC<t>{})
#define FC(s, q) fillC(IC<s>{}, IC<q>{})
  FF(0); FF(1); FF(2);
  if (threadIdx.x < 4) *(LAS unsigned*)(L0 + L_CNT + threadIdx.x * 4) = 0u;

  unsigned wa[4], xa[4];
  {
    const int lane = threadIdx.x & 63, h = lane >> 5, l31 = lane & 31, q4 = (lane & 15) >> 2, p4 = lane & 3, blk = (lane >> 4) & 1;
#pragma unroll
    for (int pos = 0; pos < 4; ++pos) {
      const int ks = pos ^ tt ^ wc;
      wa[pos] = L0 + (8 * h + q4) * 128 + ((64 * wc + 32 * blk + 8 * p4) ^ ((q4 >> 1) << 6)) + (ks << 11);
      xa[pos] = L0 + (tt >> 1) * SLOT + (32 * (tt & 1) + l31) * 128 + (((2 * ks + h) ^ ((lane >> 1) & 7)) << 4);
    }
  }
  bf16x8 xf[32];
  f32x16 acc[3];
#pragma unroll
  for (int u = 0; u < 3; ++u)
#pragma unroll
    for (int i = 0; i < 16; ++i) acc[u][i] = 0.f;
  float sx = 0.f, sxx = 0.f, cs[3] = {0.f, 0.f, 0.f};
  unsigned tag0, tag1, ep0, ep1;
  sfor<8>([&](auto S) {
    constexpr int s = S;
    SCHED();
    if constexpr (s == 0) waitbar<10>(); else if constexpr (s < 6) waitbar<5>(); else waitbar<9>();
    if constexpr (s == 0) {
      TOUCH(ep);
      ep0 = __builtin_amdgcn_readfirstlane(ep.x); ep1 = __builtin_amdgcn_readfirstlane(ep.y);
      STAMP(1);
    }
    if constexpr (s >= 1 && s <= 5) fillF(IC<(s + 2) & 7>{});
    if constexpr (s == 5) { FT(0); FT(1); }
    if constexpr (s == 6) { FT(2); FT(3); FC(0, 0); }
    if constexpr (s == 7) { FT(4); FT(5); FC(0, 1); }
    if constexpr (s > 0) {
      constexpr int d = ((s % 3) - ((s - 1) % 3)) * 5 * SLOT;
#pragma unroll
      for (int pos = 0; pos < 4; ++pos) { wa[pos] += d; xa[pos] += d; TOUCH(wa[pos]); TOUCH(xa[pos]); }
    }
    bf16x8 f[2][3];
    auto rd = [&](auto P) {
      constexpr int pos = P;
      xf[4 * s + pos] = ldsfrag(xa[pos]);
#pragma unroll
      for (int u = 0; u < 3; ++u) f[pos & 1][u] = trfrag(wa[pos] + (2 + u) * SLOT, wa[pos] + (2 + u) * SLOT + 512);
    };
    rd(IC<0>{});
    sfor<4>([&](auto P) {
      constexpr int pos = P;
      if constexpr (pos < 3) rd(IC<(pos + 1) & 3>{});
      SCHED();
#pragma unroll
      for (int u = 0; u < 3; ++u) {
        acc[u] = MFMA(f[pos & 1][u], xf[4 * s + pos], acc[u]);
        if (pos == 0) { cs[u] = dot1(f[0][u], cs[u]); TOUCH(cs[u]); }
      }
      if (pos == 1 || pos == 2) { sx = dot1(xf[4 * s + pos], sx); sxx = dotsq(xf[4 * s + pos], sxx); TOUCH(sx); TOUCH(sxx); }
      SCHED();
    });
  });
  STAMP(2);
  tag0 = ep0 * 0x9E3779B1u + 0x7F4A7C15u; tag1 = (ep1 ^ ep0) * 0x85EBCA77u + 0x165667B1u;
  const int T1 = 4 * r + tt;
  float rstd;
  unsigned mm;
  {
    const int l31 = opq_tid() & 31;
    sx = halfsum(sx); sxx = halfsum(sxx);
    *(LAS f32x2*)(L0 + L_ST + (wc * 128 + tt * 32 + l31) * 8) = (f32x2){sx, sxx};
#pragma unroll
    for (int u = 0; u < 3; ++u) {
      const float c = halfsum(cs[u]);
      *(LAS unsigned*)(L0 + L_SP + (((wc * 3 + u) * 32 + l31) * 4 + tt) * 4) = hilo(c);
    }
    SCHED();
    waitbar_l<10>();
    FT(6); FT(7); FC(0, 2);
    const f32x2 a = *(LAS f32x2*)(L0 + L_ST + (tt * 32 + l31) * 8), c = *(LAS f32x2*)(L0 + L_ST + (128 + tt * 32 + l31) * 8);
    const float mean = (a.x + c.x) * (1.0f / 512.0f);
    const float var = __builtin_fmaxf((a.y + c.y) * (1.0f / 512.0f) - mean * mean, 0.f);
    const float den = bfr(__builtin_sqrtf(bfr(bfr(var) + 1e-5f)));
    rstd = 1.0f / den;
    const float mh = -0.5f * bfr(mean);
    mm = pk2(mh, mh);
    const bf16x8 mfrag = __builtin_bit_cast(bf16x8, (v4u){mm, mm, mm, mm});
#pragma unroll
    for (int u = 0; u < 3; ++u) acc[u] = MFMA(ldsfrag(L0 + L_SP + ((wc * 3 + u) * 32 + l31) * 16), mfrag, acc[u]);
    const u64 qb0 = ws + WS_QKV + (u64)((4 * unit0 + 2 * wc) * 64 + T1) * 1024;
    const float s0 = unit0 < 8 ? rstd * 0.36067376f : rstd;
    st16<0>(l16, pack8(acc[0], 0, s0), qb0);
    st16<0>(l16, pack8(acc[0], 8, s0), qb0 + 65536);
    if (typeC) {
      st16<0>(l16, pack8(acc[1], 0, rstd), qb0 + 4 * 65536);
      st16<0>(l16, pack8(acc[1], 8, rstd), qb0 + 5 * 65536);
      st16<0>(l16, pack8(acc[2], 0, rstd), qb0 + 8 * 65536);
      st16<0>(l16, pack8(acc[2], 8, rstd), qb0 + 9 * 65536);
    } else {
      const u64 hb = ws + WS_H + (u64)(T1 * 112 + 4 * j + 2 * wc) * 1024;
      st16<0>(l16, geglu8(acc[1], acc[2], 0, rstd), hb);
      st16<1024>(l16, geglu8(acc[1], acc[2], 8, rstd), hb);
    }
  }
  STAMP(3);
  const u64 flg = ws + WS_FLAG;
  const int rC = rt >> 1, kq = w >> 1, tc = w & 1;
  v2u pv;
  unsigned poff;
  v4u kst[3], vst[3], qf;
  {
#pragma unroll
    for (int i = 0; i < 16; ++i) { acc[0][i] = 0.f; acc[1][i] = 0.f; }
    cs[0] = cs[1] = 0.f;
    bf16x8 f[2][2];
    auto rd = [&](auto I) {
      constexpr int i = I, t = i >> 2, pos = i & 3;
      constexpr int d = (TS[t] - (t == 0 ? 5 : TS[t > 0 ? t - 1 : 0])) * SLOT;
      wa[pos] += d; TOUCH(wa[pos]);
      f[i & 1][0] = trfrag(wa[pos], wa[pos] + 512);
      f[i & 1][1] = trfrag(wa[pos] + SLOT, wa[pos] + SLOT + 512);
    };
    rd(IC<0>{});
    sfor<32>([&](auto I) {
      constexpr int i = I, t = i >> 2, pos = i & 3;
      if constexpr (pos == 0 && t >= 1 && t <= 6) {
        if constexpr (t == 1) { waitbar<17>(); FC(0, 3); FC(1, 0); }
        if constexpr (t == 2) {
          waitbar<2>();
          if (threadIdx.x == 0) put_flag(flg + (u64)(r * 16 + j) * 128, tag0, tag1);
          FC(1, 1); FC(1, 2);
        }
        if constexpr (t == 3) { waitbar<16>(); FC(1, 3); FC(2, 0); }
        if constexpr (t == 4) {
          waitbar<16>(); FC(2, 1); FC(2, 2);
          const int lane = opq_tid() & 63;
          const int g4 = j >> 1;
          const int uk = 8 + g4, uv = 16 + g4;
          const int jk = uk < 12 ? uk : 12 + (uk - 12) / 3, jv = 12 + (uv - 12) / 3;
          int fr = r, fj = g4;
          if (lane >= 1 && lane <= 3) { fr = r - 3 + lane; fj = jk; }
          if (lane >= 4 && lane <= 6) { fr = r - 6 + lane; fj = jv; }
          if (lane >= 7) { fr = rC; fj = 3 * kq + (lane <= 9 ? lane - 7 : 0); }
          fr = fr < 0 ? 0 : fr;
          poff = (fr * 16 + fj) * 128;
          ld8(pv, poff, flg);
        }
        if constexpr (t == 5) { waitbar<16>(); FC(2, 3); }
        if constexpr (t == 6) {
          waitbar<1>();
          TOUCH(pv);
          poll_finish(pv, true, poff, flg, tag0, tag1);
          const int head = 2 * j + wc;
#pragma unroll
          for (int k = 0; k < 3; ++k) {
            int g = 4 * r - 8 + 3 * tt + k;
            g = g < 0 ? 0 : g;
            ld16<0>(kst[k], l16, ws + WS_QKV + (u64)((32 + head) * 64 + g) * 1024);
            ld16<0>(vst[k], l16, ws + WS_QKV + (u64)((64 + head) * 64 + g) * 1024);
          }
          ld16<0>(qf, l16, ws + WS_QKV + (u64)(head * 64 + T1) * 1024);
        }
      }
      if constexpr (i < 31) rd(IC<(i + 1) & 31>{});
      SCHED();
      acc[0] = MFMA(f[i & 1][0], xf[i], acc[0]);
      acc[1] = MFMA(f[i & 1][1], xf[i], acc[1]);
      if (pos == 0) { cs[0] = dot1(f[i & 1][0], cs[0]); cs[1] = dot1(f[i & 1][1], cs[1]); TOUCH(cs[0]); TOUCH(cs[1]); }
      SCHED();
    });
  }
  STAMP(4);
  const int TC = 2 * rt + tc;
  v4u eh[12];
  {
    const int tid = opq_tid(), lane = tid & 63, h = lane >> 5, l31 = lane & 31;
#pragma unroll
    for (int u = 0; u < 2; ++u) {
      const float c = halfsum(cs[u]);
      *(LAS unsigned*)(L0 + L_SP + (((wc * 3 + u) * 32 + l31) * 4 + tt) * 4) = hilo(c);
    }
    SCHED();
    waitbar_l<0>();
#pragma unroll
    for (int k = 0; k < 3; ++k) { TOUCH(kst[k]); TOUCH(vst[k]); }
    TOUCH(qf);
#pragma unroll
    for (int k = 0; k < 3; ++k) {
      *(LAS v4u*)(L0 + L_K + (wc * 12 + 3 * tt + k) * 1024 + l16) = kst[k];
      *(LAS v4u*)(L0 + L_V + ((3 * tt + k) * 8 + (l31 >> 2)) * 272 + (l31 & 3) * 64 + wc * 32 + h * 16) = vst[k];
    }
    if (tid < 96) *(LAS v4u*)(L0 + L_V + tid * 272 + 256) = (v4u){0x3f803f80u, 0x3f803f80u, 0x3f803f80u, 0x3f803f80u};
    {
      const u64 hb = ws + WS_H + (u64)(TC * 112 + 12 * kq) * 1024;
#pragma unroll
      for (int k = 0; k < 3; ++k) {
        ld16<0>(eh[4 * k], l16, hb + k * 4096); ld16<1024>(eh[4 * k + 1], l16, hb + k * 4096);
        ld16<2048>(eh[4 * k + 2], l16, hb + k * 4096); ld16<3072>(eh[4 * k + 3], l16, hb + k * 4096);
      }
    }
    const bf16x8 mfrag = __builtin_bit_cast(bf16x8, (v4u){mm, mm, mm, mm});
#pragma unroll
    for (int u = 0; u < 2; ++u) acc[u] = MFMA(ldsfrag(L0 + L_SP + ((wc * 3 + u) * 32 + l31) * 16), mfrag, acc[u]);
    const u64 hb = ws + WS_H + (u64)(T1 * 112 + 4 * (12 + j) + 2 * wc) * 1024;
    st16<0>(l16, geglu8(acc[0], acc[1], 0, rstd), hb);
    st16<1024>(l16, geglu8(acc[0], acc[1], 8, rstd), hb);
  }
  SCHED();
  bar_l();
  STAMP(5);
  {
    const int lane = opq_tid() & 63, h = lane >> 5, l31 = lane & 31, q4 = (lane & 15) >> 2, p4 = lane & 3, blk = (lane >> 4) & 1;
    const bf16x8 q = __builtin_bit_cast(bf16x8, qf);
    const int tmin = 8 - T1;
    const unsigned ka = L0 + L_K + (wc * 12 + tt) * 1024 + l16;
    const unsigned va = L0 + L_V + (tt * 8 + h) * 272 + (blk ? 256 + 8 * (p4 & 1) : q4 * 64 + wc * 32 + 8 * p4);
    f32x16 zero;
#pragma unroll
    for (int i = 0; i < 16; ++i) zero[i] = 0.f;
    bool dm[16];
#pragma unroll
    for (int i = 0; i < 16; ++i) dm[i] = ((i & 3) + 8 * (i >> 2) + 4 * h) <= l31;
    bf16x8 kf[9];
#pragma unroll
    for (int t = 0; t < 9; ++t) kf[t] = ldsfrag(ka + t * 1024);
    float m;
    {
      const f32x16 s8 = MFMA(kf[8], q, zero);
      f32x16 s0;
      if (tmin <= 0) s0 = MFMA(kf[0], q, zero);
      else {
#pragma unroll
        for (int i = 0; i < 16; ++i) s0[i] = -__builtin_inff();
      }
      f32x16 sm;
#pragma unroll
      for (int i = 0; i < 16; ++i) sm[i] = dm[i] ? s8[i] : s0[i];
      m = max16(sm, -__builtin_inff());
    }
#pragma unroll
    for (int t = 1; t < 8; ++t)
      if (t >= tmin) m = max16(MFMA(kf[t], q, zero), m);
    m = halfmax(m);
    waitv<0>();
    if (lane == 0) {
      const unsigned old = __hip_atomic_fetch_add((LAS unsigned*)(L0 + L_CNT), 1u, __ATOMIC_RELAXED, __HIP_MEMORY_SCOPE_WORKGROUP);
      if (old == 7u) put_flag(flg + (u64)(256 + r * 16 + j) * 128, tag0, tag1);
    }
    f32x16 nm, y;
#pragma unroll
    for (int i = 0; i < 16; ++i) { nm[i] = -m; y[i] = 0.f; }
#pragma unroll
    for (int t = 1; t < 8; ++t)
      if (t >= tmin) {
        f32x16 s = MFMA(kf[t], q, nm);
#pragma unroll
        for (int i = 0; i < 16; ++i) s[i] = __builtin_amdgcn_exp2f(s[i]);
        y = MFMA(trfrag(va + (t * 8) * 272, va + (t * 8 + 2) * 272), __builtin_bit_cast(bf16x8, pack8(s, 0, 1.0f)), y);
        y = MFMA(trfrag(va + (t * 8 + 4) * 272, va + (t * 8 + 6) * 272), __builtin_bit_cast(bf16x8, pack8(s, 8, 1.0f)), y);
      }
    poff = (256 + rC * 16 + 4 * kq + (lane & 3)) * 128;
    ld8(pv, poff, flg);
    {
      const f32x16 s8 = MFMA(kf[8], q, nm);
      f32x16 s0;
      if (tmin <= 0) s0 = MFMA(kf[0], q, nm);
      else {
#pragma unroll
        for (int i = 0; i < 16; ++i) s0[i] = -__builtin_inff();
      }
      f32x16 p8, p0;
#pragma unroll
      for (int i = 0; i < 16; ++i) {
        const float p = __builtin_amdgcn_exp2f(dm[i] ? s8[i] : s0[i]);
        p8[i] = dm[i] ? p : 0.f;
        p0[i] = dm[i] ? 0.f : p;
      }
      y = MFMA(trfrag(va + (64) * 272, va + (64 + 2) * 272), __builtin_bit_cast(bf16x8, pack8(p8, 0, 1.0f)), y);
      y = MFMA(trfrag(va + (64 + 4) * 272, va + (64 + 6) * 272), __builtin_bit_cast(bf16x8, pack8(p8, 8, 1.0f)), y);
      if (tmin <= 0) {
        y = MFMA(trfrag(va, va + 2 * 272), __builtin_bit_cast(bf16x8, pack8(p0, 0, 1.0f)), y);
        y = MFMA(trfrag(va + 4 * 272, va + 6 * 272), __builtin_bit_cast(bf16x8, pack8(p0, 8, 1.0f)), y);
      }
    }
    st16<0>(l16, pack8(y, 0, __builtin_amdgcn_rcpf(y[8])), ws + WS_O + (u64)(T1 * 32 + 2 * j + wc) * 1024);
  }
  STAMP(6);
  waitv<1>();
  TOUCH(pv);
  poll_finish(pv, true, poff, flg, tag0, tag1);
  v4u lh[16];
  {
    const u64 hb = ws + WS_H + (u64)(TC * 112 + 48 + 16 * kq) * 1024;
#pragma unroll
    for (int k = 0; k < 4; ++k) {
      ld16<0>(lh[4 * k], l16, hb + k * 4096); ld16<1024>(lh[4 * k + 1], l16, hb + k * 4096);
      ld16<2048>(lh[4 * k + 2], l16, hb + k * 4096); ld16<3072>(lh[4 * k + 3], l16, hb + k * 4096);
    }
  }
  if (b == 0 && w == 7) {
#pragma unroll 1
    for (int k = 0; k < 4; ++k) {
      v2u v;
      const unsigned o = k * 8192 + l16 * 8;
      asm volatile("global_load_dwordx2 %0, %1, %2 sc1\n\ts_waitcnt vmcnt(0)" : "=v"(v) : "v"(o), "s"(flg) : "memory");
      poll_finish(v, true, o, flg, tag0, tag1);
    }
    if (l16 == 0) __hip_atomic_store((GAS u64*)(ws + WS_EPOCH), (((u64)ep1 << 32) | ep0) + 1u, __ATOMIC_RELAXED, __HIP_MEMORY_SCOPE_AGENT);
  }
  STAMP(7);
  f32x16 ga[2], sa[2];
#pragma unroll
  for (int i = 0; i < 16; ++i) { ga[0][i] = 0.f; ga[1][i] = 0.f; sa[0][i] = 0.f; sa[1][i] = 0.f; }
  unsigned da, oa, xoff;
  {
    const int lane = opq_tid() & 63, h = lane >> 5, l31 = lane & 31, q4 = (lane & 15) >> 2, p4 = lane & 3, blk = (lane >> 4) & 1;
    const unsigned colb = (32 * blk + 8 * p4) ^ ((q4 >> 1) << 6);
    da = L0 + (4 * h + q4) * 128 + colb;
    oa = L0 + (8 * h + q4) * 128 + colb;
    xoff = (64 * rt + 32 * tc + l31) * 1024 + (64 * ct + 16 * kq + 8 * h) * 2;
  }
  v4u of[8], xr;
  SCHED();
  waitbar<20>();
#pragma unroll
  for (int k = 0; k < 12; ++k) TOUCH(eh[k]);
  asm volatile("global_load_dwordx4 %0, %1, %2" : "=v"(xr) : "v"(xoff), "s"(px) : "memory");
  FC(3, 0); FC(3, 1); FC(3, 2); FC(3, 3); FC(4, 0); FC(4, 1); FC(4, 2);
  {
    bf16x8 f[2][2];
    unsigned a0, a1;
    auto rd = [&](auto I) {
      constexpr int i = I, st = i >> 2, ks = i & 3;
      if constexpr (ks == 0) {
        constexpr unsigned pk = CS[st][0] | (CS[st][1] << 8) | (CS[st][2] << 16) | (CS[st][3] << 24);
        unsigned k8 = 8 * kq; OPQ_S(k8);
        const unsigned base = ((pk >> k8) & 255u) * SLOT;
        a0 = (st < 7 ? da : oa) + base; a1 = a0 ^ 64u;
        TOUCH(a0); TOUCH(a1);
      }
      constexpr int d = st < 7 ? 1024 : 512;
      f[i & 1][0] = trfrag(a0 + ks * 2048, a0 + ks * 2048 + d);
      f[i & 1][1] = trfrag(a1 + ks * 2048, a1 + ks * 2048 + d);
    };
    rd(IC<0>{});
    sfor<36>([&](auto I) {
      constexpr int i = I, st = i >> 2;
      if constexpr ((i & 3) == 0 && st >= 1 && st <= 7) {
        if constexpr (st == 1) {
          waitbar<24>();
          if (threadIdx.x == 0) put_flag(flg + (u64)(512 + r * 16 + j) * 128, tag0, tag1);
          FC(4, 3); FC(5, 0); FC(5, 1); FC(5, 2);
        }
        if constexpr (st == 2) {
          waitbar<7>();
#pragma unroll
          for (int k = 0; k < 16; ++k) TOUCH(lh[k]);
          TOUCH(xr);
          FC(5, 3); FC(6, 0); FC(6, 1); FC(6, 2);
        }
        if constexpr (st == 3) {
          waitbar<7>(); FC(6, 3); FC(7, 0); FC(7, 1); FC(7, 2);
          poff = (512 + rC * 16 + 4 * kq + ((l16 >> 4) & 3)) * 128;
          ld8(pv, poff, flg);
        }
        if constexpr (st == 4) { waitbar<8>(); FC(7, 3); FC(8, 0); FC(8, 1); FC(8, 2); }
        if constexpr (st == 5) {
          waitbar<4>();
          TOUCH(pv);
          poll_finish(pv, true, poff, flg, tag0, tag1);
          FC(8, 3);
          const u64 ob = ws + WS_O + (u64)(TC * 32 + 8 * kq) * 1024;
#pragma unroll
          for (int k = 0; k < 2; ++k) {
            ld16<0>(of[4 * k], l16, ob + k * 4096); ld16<1024>(of[4 * k + 1], l16, ob + k * 4096);
            ld16<2048>(of[4 * k + 2], l16, ob + k * 4096); ld16<3072>(of[4 * k + 3], l16, ob + k * 4096);
          }
        }
        if constexpr (st == 6) waitbar<12>();
        if constexpr (st == 7) {
          waitbar<0>();
#pragma unroll
          for (int k = 0; k < 8; ++k) TOUCH(of[k]);
        }
      }
      if constexpr (i < 35) rd(IC<(i + 1 < 36 ? i + 1 : 0)>{});
      SCHED();
      v4u au;
      if constexpr (i < 12) au = eh[i < 12 ? i : 0]; else if constexpr (i < 28) au = lh[i < 28 && i >= 12 ? i - 12 : 0]; else au = of[i >= 28 ? i - 28 : 0];
      const bf16x8 a = __builtin_bit_cast(bf16x8, au);
      if constexpr (i < 28) { ga[0] = MFMA(f[i & 1][0], a, ga[0]); ga[1] = MFMA(f[i & 1][1], a, ga[1]); }
      else { sa[0] = MFMA(f[i & 1][0], a, sa[0]); sa[1] = MFMA(f[i & 1][1], a, sa[1]); }
      SCHED();
    });
  }
  STAMP(8);
  bar_l();
  {
    unsigned rw = L0 + tc * 32768 + kq * 2048 + l16, rw2 = rw + 65536;
    TOUCH(rw); TOUCH(rw2);
#pragma unroll
    for (int d = 0; d < 4; ++d) {
#pragma unroll
      for (int k = 0; k < 2; ++k) {
        const int o = 8 * (d & 1) + 4 * k;
        *(LAS f32x4*)(rw + d * 8192 + k * 1024) = (f32x4){ga[d >> 1][o], ga[d >> 1][o + 1], ga[d >> 1][o + 2], ga[d >> 1][o + 3]};
        *(LAS f32x4*)(rw2 + d * 8192 + k * 1024) = (f32x4){sa[d >> 1][o], sa[d >> 1][o + 1], sa[d >> 1][o + 2], sa[d >> 1][o + 3]};
      }
    }
    bar_l();
    unsigned rr = L0 + tc * 32768 + kq * 8192 + l16, rr2 = rr + 65536;
    TOUCH(rr); TOUCH(rr2);
    f32x4 g2[2], s2[2];
#pragma unroll
    for (int k = 0; k < 2; ++k) {
      g2[k] = *(LAS f32x4*)(rr + k * 1024);
      s2[k] = *(LAS f32x4*)(rr2 + k * 1024);
#pragma unroll
      for (int s = 1; s < 4; ++s) {
        g2[k] += *(LAS f32x4*)(rr + s * 2048 + k * 1024);
        s2[k] += *(LAS f32x4*)(rr2 + s * 2048 + k * 1024);
      }
    }
    float yv[8];
#pragma unroll
    for (int i = 0; i < 4; ++i) {
      const float a = s2[0][i] * g2[0][i] * __builtin_amdgcn_rcpf(1.0f + __builtin_amdgcn_exp2f(-1.44269504f * g2[0][i]));
      const float c = s2[1][i] * g2[1][i] * __builtin_amdgcn_rcpf(1.0f + __builtin_amdgcn_exp2f(-1.44269504f * g2[1][i]));
      const auto sw = __builtin_amdgcn_permlane32_swap(__builtin_bit_cast(unsigned, a), __builtin_bit_cast(unsigned, c), false, false);
      const unsigned sw0 = sw[0], sw1 = sw[1];
      yv[i] = __builtin_bit_cast(float, sw0);
      yv[4 + i] = __builtin_bit_cast(float, sw1);
    }
    v4u o;
#pragma unroll
    for (int i = 0; i < 4; ++i) {
      const unsigned xi = xr[i];
      o[i] = pk2(__builtin_bit_cast(float, xi << 16) + yv[2 * i], __builtin_bit_cast(float, xi & 0xffff0000u) + yv[2 * i + 1]);
    }
    *(GAS v4u*)(pout + xoff) = o;
  }
  STAMP(9);
  asm volatile(".Lfb_end:");
}

extern "C" void kernel_launch(void* const* d_in, const int* in_sizes, int n_in, void* d_out, int out_size, void* d_ws, size_t ws_size,
                              hipStream_t stream) {
  fused_block<<<256, 512, 0, stream>>>((const char*)d_in[0], (const char*)d_in[1], (const char*)d_in[2], (const char*)d_in[4],
                                       (const char*)d_in[5], (const char*)d_in[6], (char*)d_out, (char*)d_ws);
}
```

```cpp
#include <hip/hip_runtime.h>
#include <hip/hip_bf16.h>
#include <utility>

typedef unsigned v4u __attribute__((ext_vector_type(4)));
typedef unsigned v2u __attribute__((ext_vector_type(2)));
typedef float f32x2 __attribute__((ext_vector_type(2)));
typedef float f32x4 __attribute__((ext_vector_type(4)));
typedef float f32x16 __attribute__((ext_vector_type(16)));
typedef __bf16 bf16x2 __attribute__((ext_vector_type(2)));
typedef __bf16 bf16x8 __attribute__((ext_vector_type(8)));
typedef short s16x4 __attribute__((ext_vector_type(4)));
typedef unsigned long long u64;
#define LAS __attribute__((address_space(3)))
#define GAS __attribute__((address_space(1)))
#define DI __device__ __forceinline__

#define ROT 0
#define LA 2
#define LA1 1

constexpr u64 WS_EPOCH = 0;
constexpr u64 WS_FLAG = 4096;
constexpr u64 WS_QKV = 1u << 20;
constexpr u64 WS_H = WS_QKV + (6u << 20);
constexpr u64 WS_O = WS_H + (7u << 20);
constexpr u64 WS_STAMP = 32u << 20;
constexpr int SLOT = 8192;
constexpr int MISC = 19 * SLOT;
constexpr int L_SP = MISC;
constexpr int L_ST = MISC + 3072;
constexpr int L_CNT = MISC + 5120;
constexpr int L_SCR = MISC + 6144;
constexpr int L_K = 1 * SLOT;
constexpr int L_V = 5 * SLOT;
constexpr int LDS_BYTES = 20 * SLOT;

#define MFMA(a, b, c) __builtin_amdgcn_mfma_f32_32x32x16_bf16((a), (b), (c), 0, 0, 0)
#define SCHED() __builtin_amdgcn_sched_barrier(0)

template <int LIMM> DI void glds(unsigned voff, u64 sbase, unsigned soff, unsigned lbase) {
  asm volatile("s_add_u32 m0, %4, %5\n\ts_add_u32 s90, %1, %3\n\ts_addc_u32 s91, %2, 0\n\tglobal_load_lds_dwordx4 %0, s[90:91]"
               ::"v"(voff), "s"((unsigned)sbase), "s"((unsigned)(sbase >> 32)), "s"(soff), "s"(lbase), "n"(LIMM)
               : "memory", "scc", "s90", "s91");
}
template <int LIMM, int SIMM> DI void gldsi(unsigned voff, u64 sbase, unsigned lbase) {
  asm volatile("s_add_u32 m0, %3, %4\n\ts_add_u32 s90, %1, %5\n\ts_addc_u32 s91, %2, 0\n\tglobal_load_lds_dwordx4 %0, s[90:91]"
               ::"v"(voff), "s"((unsigned)sbase), "s"((unsigned)(sbase >> 32)), "s"(lbase), "n"(LIMM), "n"(SIMM)
               : "memory", "scc", "s90", "s91");
}
#define OPQ_S(x) asm volatile("" : "+s"(x))
template <int N> DI void waitbar() { asm volatile("s_waitcnt vmcnt(%0)\n\ts_barrier" ::"n"(N) : "memory"); }
template <int N> DI void waitbar_l() { asm volatile("s_waitcnt vmcnt(%0) lgkmcnt(0)\n\ts_barrier" ::"n"(N) : "memory"); }
DI void bar_l() { asm volatile("s_waitcnt lgkmcnt(0)\n\ts_barrier" ::: "memory"); }
template <int N> DI void waitv() { asm volatile("s_waitcnt vmcnt(%0)" ::"n"(N) : "memory"); }
template <int OFF> DI void ld16(v4u& d, unsigned voff, u64 sbase) {
  asm volatile("global_load_dwordx4 %0, %1, %2 offset:%3 sc1" : "=v"(d) : "v"(voff), "s"(sbase), "n"(OFF) : "memory");
}
DI void ld16p(v4u& d, unsigned voff, u64 sbase) {
  asm volatile("global_load_dwordx4 %0, %1, %2" : "=v"(d) : "v"(voff), "s"(sbase) : "memory");
}
template <int OFF> DI void st16(unsigned voff, v4u d, u64 sbase) {
  asm volatile("global_store_dwordx4 %0, %1, %2 offset:%3 sc1\n\ts_nop 1" ::"v"(voff), "v"(d), "s"(sbase), "n"(OFF) : "memory");
}
DI void ld8(v2u& d, unsigned voff, u64 sbase) {
  asm volatile("global_load_dwordx2 %0, %1, %2 sc1" : "=v"(d) : "v"(voff), "s"(sbase) : "memory");
}
#define TOUCH(x) asm volatile("" : "+v"(x))

DI bf16x8 trfrag(unsigned a0, unsigned a1) {
  const s16x4 lo = __builtin_amdgcn_ds_read_tr16_b64_v4i16((LAS s16x4*)(size_t)a0);
  const s16x4 hi = __builtin_amdgcn_ds_read_tr16_b64_v4i16((LAS s16x4*)(size_t)a1);
  return __builtin_bit_cast(bf16x8, __builtin_shufflevector(lo, hi, 0, 1, 2, 3, 4, 5, 6, 7));
}
DI bf16x8 ldsfrag(unsigned a) { return *(LAS bf16x8*)(size_t)a; }
DI unsigned pk2(float a, float b) { return __builtin_bit_cast(unsigned, __builtin_convertvector((f32x2){a, b}, bf16x2)); }
DI float bfr(float a) { return __builtin_bit_cast(float, pk2(a, a) & 0xffff0000u); }
DI float dot1(bf16x8 f, float acc) {
  const v4u u = __builtin_bit_cast(v4u, f);
  const bf16x2 one = {(__bf16)1.0f, (__bf16)1.0f};
#pragma unroll
  for (int i = 0; i < 4; ++i) { const unsigned e = u[i]; acc = __builtin_amdgcn_fdot2_f32_bf16(__builtin_bit_cast(bf16x2, e), one, acc, false); }
  return acc;
}
DI float dotsq(bf16x8 f, float acc) {
  const v4u u = __builtin_bit_cast(v4u, f);
#pragma unroll
  for (int i = 0; i < 4; ++i) { const unsigned e = u[i]; acc = __builtin_amdgcn_fdot2_f32_bf16(__builtin_bit_cast(bf16x2, e), __builtin_bit_cast(bf16x2, e), acc, false); }
  return acc;
}
DI float halfsum(float v) {
  const unsigned u = __builtin_bit_cast(unsigned, v);
  const auto r = __builtin_amdgcn_permlane32_swap(u, u, false, false);
  const unsigned r0 = r[0], r1 = r[1];
  return __builtin_bit_cast(float, r0) + __builtin_bit_cast(float, r1);
}
DI float halfmax(float v) {
  const unsigned u = __builtin_bit_cast(unsigned, v);
  const auto r = __builtin_amdgcn_permlane32_swap(u, u, false, false);
  const unsigned r0 = r[0], r1 = r[1];
  return fmaxf(__builtin_bit_cast(float, r0), __builtin_bit_cast(float, r1));
}
DI v4u pack8(const f32x16& a, int o, float s) {
  return (v4u){pk2(a[o] * s, a[o + 1] * s), pk2(a[o + 2] * s, a[o + 3] * s), pk2(a[o + 4] * s, a[o + 5] * s), pk2(a[o + 6] * s, a[o + 7] * s)};
}
DI float geglu(float ga, float ua, float rs) {
  const float g = ga * rs;
  const float e = __builtin_amdgcn_exp2f(g * __builtin_fmaf(g * g, -0.10294324f, -2.3022082f));
  return (g * ua) * (__builtin_amdgcn_rcpf(1.0f + e) * rs);
}
DI v4u geglu8(const f32x16& g, const f32x16& u, int o, float rs) {
  float h[8];
#pragma unroll
  for (int i = 0; i < 8; ++i) h[i] = geglu(g[o + i], u[o + i], rs);
  return (v4u){pk2(h[0], h[1]), pk2(h[2], h[3]), pk2(h[4], h[5]), pk2(h[6], h[7])};
}
DI unsigned hilo(float s) {
  const float hi = bfr(s);
  return pk2(hi, s - hi);
}
DI float max16(const f32x16& s, float m) {
#pragma unroll
  for (int i = 0; i < 16; i += 2) m = __builtin_fmaxf(__builtin_fmaxf(m, s[i]), s[i + 1]);
  return m;
}
DI void poll_finish(v2u v, bool act, unsigned voff, u64 sbase, unsigned t0, unsigned t1) {
#pragma unroll 1
  for (unsigned spins = 0; spins < (1u << 14); ++spins) {
    const bool ok = !act || (v.x == t0 && v.y == t1);
    if (__builtin_amdgcn_ballot_w64(ok) == ~0ull) break;
    __builtin_amdgcn_s_sleep(2);
    asm volatile("global_load_dwordx2 %0, %1, %2 sc1\n\ts_waitcnt vmcnt(0)" : "=v"(v) : "v"(voff), "s"(sbase) : "memory");
  }
}
template <int I> using IC = std::integral_constant<int, I>;
template <class F, int... Is> DI void sfor_impl(F&& f, std::integer_sequence<int, Is...>) { (f(IC<Is>{}), ...); }
template <int N, class F> DI void sfor(F&& f) { sfor_impl(f, std::make_integer_sequence<int, N>{}); }
constexpr int TS[8] = {15, 17, 10, 12, 0, 2, 5, 7};
constexpr int CS[9][4] = {{14, 4, 9, 15}, {16, 17, 18, 10}, {11, 12, 13, 0}, {5, 6, 7, 8}, {1, 2, 3, 14}, {4, 9, 15, 16}, {17, 18, 10, 11}, {12, 13, 0, 5}, {6, 7, 8, 1}};
DI int opq_tid() { int t = threadIdx.x; asm volatile("" : "+v"(t)); return t; }
DI void put_flag(u64 a, unsigned t0, unsigned t1) {
  __hip_atomic_store((GAS u64*)a, (u64)t0 | ((u64)t1 << 32), __ATOMIC_RELAXED, __HIP_MEMORY_SCOPE_AGENT);
}
#define STAMP(i)
#define FS(i)

__global__ void __launch_bounds__(512, 2)
fused_block(const char* __restrict__ xg, const char* __restrict__ wqkv, const char* __restrict__ wo, const char* __restrict__ wg,
            const char* __restrict__ wu, const char* __restrict__ wd, char* __restrict__ outg, char* __restrict__ wsp) {
  __shared__ __attribute__((aligned(1024))) char lds[LDS_BYTES];
  u64 px = (u64)xg, pq = (u64)wqkv, po = (u64)wo, pg = (u64)wg, pu = (u64)wu, pd = (u64)wd, pout = (u64)outg, ws = (u64)wsp;
  asm volatile("" : "+s"(px), "+s"(pq), "+s"(po), "+s"(pg), "+s"(pu), "+s"(pd), "+s"(pout), "+s"(ws));
  const int w = __builtin_amdgcn_readfirstlane(threadIdx.x >> 6);
  const int b = blockIdx.x, xcd = b & 7, idx = b >> 3;
  const int j = 4 * (xcd & 3) + (idx & 3), r = 8 * (xcd >> 2) + (idx >> 2);
  const int rt = 4 * xcd + (idx & 3), ct = idx >> 2;
  const bool typeC = j >= 12;
  const unsigned L0 = (unsigned)(size_t)(LAS char*)lds;
  const unsigned w1k = L0 + w * 1024;
  const int tt = w & 3, wc = w >> 2;
  const unsigned l16 = (threadIdx.x & 63) * 16;
  STAMP(0);
  FS(0);

  if (w == 0) {
    u64 pc;
    unsigned len;
    asm volatile("s_getpc_b64 %0\n.Lfb_here:\n\ts_mov_b32 %1, .Lfb_end-.Lfb_here" : "=s"(pc), "=s"(len));
    if (idx * 1024 + 1024 <= len) glds<L_SCR>(l16, pc & ~(u64)127, idx * 1024, L0);
  }
  v2u ep;
  ld8(ep, 0u, ws + WS_EPOCH);
  const int rot = ROT ? ((r + 2 * (idx & 3)) & 7) : 0;
  const int unit0 = typeC ? 3 * j - 24 : j;
  unsigned vox, voq, vog, voc, vo12;
  {
    const int lane = threadIdx.x & 63;
    const unsigned rowi = 8 * w + (lane >> 3);
    const unsigned wsw = ((lane & 7) ^ (((lane >> 4) & 1) << 2)) * 16;
    vox = rowi * 1024 + (((lane & 7) ^ ((4 * (w & 1) + (lane >> 4)) & 7)) * 16);
    voq = rowi * 3072 + wsw; vog = rowi * 3584 + wsw; voc = rowi * 1024 + wsw;
    vo12 = typeC ? voq : vog;
  }
  const u64 bx = px + (u64)r * 131072;
  const u64 bu0 = pq + unit0 * 128;
  const u64 bu1 = typeC ? bu0 + 128 : pg + j * 128;
  const u64 bu2 = typeC ? bu0 + 256 : pu + j * 128;
  const unsigned cs12 = typeC ? 64 * 3072 : 64 * 3584;
  const u64 bwd = pd + ct * 128, bwo = po + ct * 128;

  auto fillF1 = [&](auto S, auto K) {
    constexpr int s = S, k = K, sl = (s % 3) * 5 * SLOT;
    unsigned ro = rot; OPQ_S(ro);
    const unsigned kc = (s + ro) & 7;
    if constexpr (k == 0) glds<sl>(vox, bx, kc * 128, w1k);
    if constexpr (k == 1) glds<sl + SLOT>(vox, bx, kc * 128 + 65536, w1k);
    if constexpr (k == 2) glds<sl + 2 * SLOT>(voq, bu0, kc * (64 * 3072), w1k);
    if constexpr (k == 3) glds<sl + 3 * SLOT>(vo12, bu1, kc * cs12, w1k);
    if constexpr (k == 4) glds<sl + 4 * SLOT>(vo12, bu2, kc * cs12, w1k);
  };
  auto fillF = [&](auto S) { sfor<5>([&](auto K) { fillF1(S, K); }); };
  auto fillT1 = [&](auto T, auto K) {
    constexpr int t = T, k = K;
    unsigned ro = rot; OPQ_S(ro);
    const unsigned o = ((t + ro) & 7) * (64 * 3584) + (12 + j) * 128;
    glds<(TS[t] + k) * SLOT>(vog, k ? pu : pg, o, w1k);
  };
  auto fillT = [&](auto T) { fillT1(T, IC<0>{}); fillT1(T, IC<1>{}); };
  auto fillC = [&](auto ST, auto Q) {
    constexpr int st = ST, q = Q;
    if constexpr (st < 3) gldsi<CS[st][q] * SLOT, (3 * q + st) * 65536>(voc, bwd, w1k);
    else if constexpr (st < 7) gldsi<CS[st][q] * SLOT, (12 + 4 * q + st - 3) * 65536>(voc, bwd, w1k);
    else gldsi<CS[st][q] * SLOT, (2 * q + st - 7) * 65536>(voc, bwo, w1k);
  };
#define FF(s) fillF(IC<(s)>{})
#define FT(t) fillT(IC<(t)>{})
#define FC(s, q) fillC(IC<(s)>{}, IC<(q)>{})
#define FF1(s, k) fillF1(IC<(s)>{}, IC<(k)>{})
#define FT1(t, k) fillT1(IC<(t)>{}, IC<(k)>{})
  FF(0); FF(1); FF(2);
  if (threadIdx.x < 4) *(LAS unsigned*)(L0 + L_CNT + threadIdx.x * 4) = 0u;

  unsigned wa[4], xa[4];
  {
    const int lane = threadIdx.x & 63, h = lane >> 5, l31 = lane & 31, q4 = (lane & 15) >> 2, p4 = lane & 3, blk = (lane >> 4) & 1;
#pragma unroll
    for (int pos = 0; pos < 4; ++pos) {
      const int ks = pos ^ tt ^ wc;
      wa[pos] = L0 + (8 * h + q4) * 128 + ((64 * wc + 32 * blk + 8 * p4) ^ ((q4 >> 1) << 6)) + (ks << 11);
      xa[pos] = L0 + (tt >> 1) * SLOT + (32 * (tt & 1) + l31) * 128 + (((2 * ks + h) ^ ((lane >> 1) & 7)) << 4);
    }
  }
  bf16x8 xf[32];
  f32x16 acc[3];
#pragma unroll
  for (int u = 0; u < 3; ++u)
#pragma unroll
    for (int i = 0; i < 16; ++i) acc[u][i] = 0.f;
  float sx = 0.f, sxx = 0.f, cs[3] = {0.f, 0.f, 0.f};
  unsigned tag0, tag1, ep0, ep1;
  sfor<8>([&](auto S) {
    constexpr int s = S;
    SCHED();
    FS(1 + 2 * s);
    if constexpr (s == 0) waitbar<10>(); else if constexpr (s < 6) waitbar<5>(); else waitbar<9>();
    FS(2 + 2 * s);
    if constexpr (s == 0) {
      TOUCH(ep);
      ep0 = __builtin_amdgcn_readfirstlane(ep.x); ep1 = __builtin_amdgcn_readfirstlane(ep.y);
      STAMP(1);
    }
    if constexpr (s > 0) {
      constexpr int d = ((s % 3) - ((s - 1) % 3)) * 5 * SLOT;
#pragma unroll
      for (int pos = 0; pos < 4; ++pos) { wa[pos] += d; xa[pos] += d; TOUCH(wa[pos]); TOUCH(xa[pos]); }
    }
    bf16x8 f[LA1 + 1][3];
    auto rd = [&](auto P) {
      constexpr int pos = P;
      xf[4 * s + pos] = ldsfrag(xa[pos]);
#pragma unroll
      for (int u = 0; u < 3; ++u) f[pos % (LA1 + 1)][u] = trfrag(wa[pos] + (2 + u) * SLOT, wa[pos] + (2 + u) * SLOT + 512);
    };
    sfor<LA1>([&](auto P) { rd(P); });
    sfor<4>([&](auto P) {
      constexpr int pos = P;
      if constexpr (pos + LA1 < 4) rd(IC<(pos + LA1) & 3>{});
      SCHED();
#pragma unroll
      for (int u = 0; u < 3; ++u) {
        acc[u] = MFMA(f[pos % (LA1 + 1)][u], xf[4 * s + pos], acc[u]);
        if (pos == 0) { cs[u] = dot1(f[0][u], cs[u]); TOUCH(cs[u]); }
      }
      if (pos == 1 || pos == 2) { sx = dot1(xf[4 * s + pos], sx); sxx = dotsq(xf[4 * s + pos], sxx); TOUCH(sx); TOUCH(sxx); }
      SCHED();
      constexpr int n = (s + 2) & 7;
      if constexpr (s >= 1 && s <= 4) {
        if constexpr (pos == 0) { FF1(n, 0); FF1(n, 1); } else FF1(n, pos + 1);
      }
      if constexpr (s == 5) {
        if constexpr (pos == 0) { FF1(7, 0); FF1(7, 1); FF1(7, 2); }
        if constexpr (pos == 1) { FF1(7, 3); FF1(7, 4); }
        if constexpr (pos == 2) FT(0);
        if constexpr (pos == 3) FT(1);
      }
      if constexpr (s == 6 || s == 7) {
        constexpr int t = 2 * s - 10;
        if constexpr (pos == 0) FT(t);
        if constexpr (pos == 1) FT1(t + 1, 0);
        if constexpr (pos == 2) FT1(t + 1, 1);
        if constexpr (pos == 3) FC(0, s - 6);
      }
    });
  });
  STAMP(2);
  tag0 = ep0 * 0x9E3779B1u + 0x7F4A7C15u; tag1 = (ep1 ^ ep0) * 0x85EBCA77u + 0x165667B1u;
  const int T1 = 4 * r + tt;
  float rstd;
  unsigned mm;
  {
    const int l31 = opq_tid() & 31;
    sx = halfsum(sx); sxx = halfsum(sxx);
    *(LAS f32x2*)(L0 + L_ST + (wc * 128 + tt * 32 + l31) * 8) = (f32x2){sx, sxx};
#pragma unroll
    for (int u = 0; u < 3; ++u) {
      const float c = halfsum(cs[u]);
      *(LAS unsigned*)(L0 + L_SP + (((wc * 3 + u) * 32 + l31) * 4 + tt) * 4) = hilo(c);
    }
    SCHED();
    FS(17);
    waitbar_l<10>();
    FS(18);
    FT(6); FT(7); FC(0, 2);
    const f32x2 a = *(LAS f32x2*)(L0 + L_ST + (tt * 32 + l31) * 8), c = *(LAS f32x2*)(L0 + L_ST + (128 + tt * 32 + l31) * 8);
    const float mean = (a.x + c.x) * (1.0f / 512.0f);
    const float var = __builtin_fmaxf((a.y + c.y) * (1.0f / 512.0f) - mean * mean, 0.f);
    const float den = bfr(__builtin_sqrtf(bfr(bfr(var) + 1e-5f)));
    rstd = 1.0f / den;
    const float mh = -0.5f * bfr(mean);
    mm = pk2(mh, mh);
    const bf16x8 mfrag = __builtin_bit_cast(bf16x8, (v4u){mm, mm, mm, mm});
#pragma unroll
    for (int u = 0; u < 3; ++u) acc[u] = MFMA(ldsfrag(L0 + L_SP + ((wc * 3 + u) * 32 + l31) * 16), mfrag, acc[u]);
    const u64 qb0 = ws + WS_QKV + (u64)((4 * unit0 + 2 * wc) * 64 + T1) * 1024;
    const float s0 = unit0 < 8 ? rstd * 0.36067376f : rstd;
    st16<0>(l16, pack8(acc[0], 0, s0), qb0);
    st16<0>(l16, pack8(acc[0], 8, s0), qb0 + 65536);
    if (typeC) {
      st16<0>(l16, pack8(acc[1], 0, rstd), qb0 + 4 * 65536);
      st16<0>(l16, pack8(acc[1], 8, rstd), qb0 + 5 * 65536);
      st16<0>(l16, pack8(acc[2], 0, rstd), qb0 + 8 * 65536);
      st16<0>(l16, pack8(acc[2], 8, rstd), qb0 + 9 * 65536);
    } else {
      const u64 hb = ws + WS_H + (u64)(T1 * 112 + 4 * j + 2 * wc) * 1024;
      st16<0>(l16, geglu8(acc[1], acc[2], 0, rstd), hb);
      st16<1024>(l16, geglu8(acc[1], acc[2], 8, rstd), hb);
    }
  }
  STAMP(3);
  const u64 flg = ws + WS_FLAG;
  const int rC = rt >> 1, kq = w >> 1, tc = w & 1;
  v2u pv;
  unsigned poff;
  v4u kst[3], vst[3], qf;
  {
#pragma unroll
    for (int i = 0; i < 16; ++i) { acc[0][i] = 0.f; acc[1][i] = 0.f; }
    cs[0] = cs[1] = 0.f;
    bf16x8 f[LA + 1][2];
    auto rd = [&](auto I) {
      constexpr int i = I, t = i >> 2, pos = i & 3;
      constexpr int d = (TS[t] - (t == 0 ? 5 : TS[t > 0 ? t - 1 : 0])) * SLOT;
      wa[pos] += d; TOUCH(wa[pos]);
      f[i % (LA + 1)][0] = trfrag(wa[pos], wa[pos] + 512);
      f[i % (LA + 1)][1] = trfrag(wa[pos] + SLOT, wa[pos] + SLOT + 512);
    };
    sfor<LA>([&](auto I) { rd(I); });
    const int head = 2 * j + wc;
    sfor<32>([&](auto I) {
      constexpr int i = I, t = i >> 2, pos = i & 3;
      if constexpr (i == 0) FS(19);
      if constexpr (pos == 0 && t >= 1 && t <= 6) {
        FS(18 + 2 * t);
        if constexpr (t == 1) waitbar<17>();
        if constexpr (t == 2) {
          waitbar<2>();
          if (threadIdx.x == 0) put_flag(flg + (u64)(r * 16 + j) * 128, tag0, tag1);
        }
        if constexpr (t == 3) waitbar<16>();
        if constexpr (t == 4) {
          waitbar<16>();
          const int lane = opq_tid() & 63;
          const int g4 = j >> 1;
          const int uk = 8 + g4, uv = 16 + g4;
          const int jk = uk < 12 ? uk : 12 + (uk - 12) / 3, jv = 12 + (uv - 12) / 3;
          int fr = r, fj = g4;
          if (lane >= 1 && lane <= 3) { fr = r - 3 + lane; fj = jk; }
          if (lane >= 4 && lane <= 6) { fr = r - 6 + lane; fj = jv; }
          if (lane >= 7) { fr = rC; fj = 3 * kq + (lane <= 9 ? lane - 7 : 0); }
          fr = fr < 0 ? 0 : fr;
          poff = (fr * 16 + fj) * 128;
          ld8(pv, poff, flg);
        }
        if constexpr (t == 5) waitbar<16>();
        if constexpr (t == 6) {
          waitbar<3>();
          TOUCH(pv);
          poll_finish(pv, true, poff, flg, tag0, tag1);
        }
        FS(19 + 2 * t);
      }
      if constexpr (i + LA < 32) rd(IC<(i + LA) & 31>{});
      SCHED();
      acc[0] = MFMA(f[i % (LA + 1)][0], xf[i], acc[0]);
      acc[1] = MFMA(f[i % (LA + 1)][1], xf[i], acc[1]);
      if (pos == 0) { cs[0] = dot1(f[i % (LA + 1)][0], cs[0]); cs[1] = dot1(f[i % (LA + 1)][1], cs[1]); TOUCH(cs[0]); TOUCH(cs[1]); }
      SCHED();
      if constexpr (t >= 1 && t <= 5 && (pos == 0 || pos == 2)) {
        constexpr int n = 3 + 2 * (t - 1) + (pos >> 1);
        if constexpr (n < 12) FC(n >> 2, n & 3);
      }
      if constexpr (i >= 24 && i < 30) {
        constexpr int k = (i - 24) >> 1;
        int g = 4 * r - 8 + 3 * tt + k;
        g = g < 0 ? 0 : g;
        if constexpr (i & 1) ld16<0>(vst[k], l16, ws + WS_QKV + (u64)((64 + head) * 64 + g) * 1024);
        else ld16<0>(kst[k], l16, ws + WS_QKV + (u64)((32 + head) * 64 + g) * 1024);
      }
      if constexpr (i == 30) ld16<0>(qf, l16, ws + WS_QKV + (u64)(head * 64 + T1) * 1024);
    });
  }
  STAMP(4);
  const int TC = 2 * rt + tc;
  v4u eh[12];
  {
    const int tid = opq_tid(), lane = tid & 63, h = lane >> 5, l31 = lane & 31;
#pragma unroll
    for (int u = 0; u < 2; ++u) {
      const float c = halfsum(cs[u]);
      *(LAS unsigned*)(L0 + L_SP + (((wc * 3 + u) * 32 + l31) * 4 + tt) * 4) = hilo(c);
    }
    SCHED();
    FS(32);
    bar_l();
    FS(33);
    const bf16x8 mfrag = __builtin_bit_cast(bf16x8, (v4u){mm, mm, mm, mm});
#pragma unroll
    for (int u = 0; u < 2; ++u) acc[u] = MFMA(ldsfrag(L0 + L_SP + ((wc * 3 + u) * 32 + l31) * 16), mfrag, acc[u]);
    const u64 hb = ws + WS_H + (u64)(T1 * 112 + 4 * (12 + j) + 2 * wc) * 1024;
    const u64 eb = ws + WS_H + (u64)(TC * 112 + 12 * kq) * 1024;
    v4u hv;
    sfor<8>([&](auto G) {
      constexpr int g = G;
      hv[g & 3] = pk2(geglu(acc[0][2 * g], acc[1][2 * g], rstd), geglu(acc[0][2 * g + 1], acc[1][2 * g + 1], rstd));
      TOUCH(hv[g & 3]);
      SCHED();
      if constexpr (g < 6) {
        ld16<((2 * g) & 3) * 1024>(eh[2 * g], l16, eb + ((2 * g) >> 2) * 4096);
        ld16<((2 * g + 1) & 3) * 1024>(eh[2 * g + 1], l16, eb + ((2 * g + 1) >> 2) * 4096);
      }
      if constexpr (g == 3) st16<0>(l16, hv, hb);
      if constexpr (g == 7) st16<1024>(l16, hv, hb);
    });
    FS(34);
    waitv<14>();
    FS(35);
#pragma unroll
    for (int k = 0; k < 3; ++k) { TOUCH(kst[k]); TOUCH(vst[k]); }
    TOUCH(qf);
#pragma unroll
    for (int k = 0; k < 3; ++k) {
      *(LAS v4u*)(L0 + L_K + (wc * 12 + 3 * tt + k) * 1024 + l16) = kst[k];
      *(LAS v4u*)(L0 + L_V + ((3 * tt + k) * 8 + (l31 >> 2)) * 272 + (l31 & 3) * 64 + wc * 32 + h * 16) = vst[k];
    }
    if (tid < 96) *(LAS v4u*)(L0 + L_V + tid * 272 + 256) = (v4u){0x3f803f80u, 0x3f803f80u, 0x3f803f80u, 0x3f803f80u};
  }
  SCHED();
  FS(36);
  bar_l();
  FS(37);
  STAMP(5);
  {
    const int lane = opq_tid() & 63, h = lane >> 5, l31 = lane & 31, q4 = (lane & 15) >> 2, p4 = lane & 3, blk = (lane >> 4) & 1;
    const bf16x8 q = __builtin_bit_cast(bf16x8, qf);
    const int tmin = 8 - T1;
    const unsigned ka = L0 + L_K + (wc * 12 + tt) * 1024 + l16;
    const unsigned va = L0 + L_V + (tt * 8 + h) * 272 + (blk ? 256 + 8 * (p4 & 1) : q4 * 64 + wc * 32 + 8 * p4);
    f32x16 zero;
#pragma unroll
    for (int i = 0; i < 16; ++i) zero[i] = 0.f;
    bool dm[16];
#pragma unroll
    for (int i = 0; i < 16; ++i) dm[i] = ((i & 3) + 8 * (i >> 2) + 4 * h) <= l31;
    bf16x8 kf[9];
#pragma unroll
    for (int t = 0; t < 9; ++t) kf[t] = ldsfrag(ka + t * 1024);
    float m;
    {
      const f32x16 s8 = MFMA(kf[8], q, zero);
      f32x16 s0;
      if (tmin <= 0) s0 = MFMA(kf[0], q, zero);
      else {
#pragma unroll
        for (int i = 0; i < 16; ++i) s0[i] = -__builtin_inff();
      }
      f32x16 sm;
#pragma unroll
      for (int i = 0; i < 16; ++i) sm[i] = dm[i] ? s8[i] : s0[i];
      m = max16(sm, -__builtin_inff());
    }
#pragma unroll
    for (int t = 1; t < 8; ++t)
      if (t >= tmin) m = max16(MFMA(kf[t], q, zero), m);
    m = halfmax(m);
    FS(38);
    waitv<0>();
    FS(39);
    if (lane == 0) {
      const unsigned old = __hip_atomic_fetch_add((LAS unsigned*)(L0 + L_CNT), 1u, __ATOMIC_RELAXED, __HIP_MEMORY_SCOPE_WORKGROUP);
      if (old == 7u) put_flag(flg + (u64)(256 + r * 16 + j) * 128, tag0, tag1);
    }
    f32x16 nm, y;
#pragma unroll
    for (int i = 0; i < 16; ++i) { nm[i] = -m; y[i] = 0.f; }
#pragma unroll
    for (int t = 1; t < 8; ++t)
      if (t >= tmin) {
        f32x16 s = MFMA(kf[t], q, nm);
#pragma unroll
        for (int i = 0; i < 16; ++i) s[i] = __builtin_amdgcn_exp2f(s[i]);
        y = MFMA(trfrag(va + (t * 8) * 272, va + (t * 8 + 2) * 272), __builtin_bit_cast(bf16x8, pack8(s, 0, 1.0f)), y);
        y = MFMA(trfrag(va + (t * 8 + 4) * 272, va + (t * 8 + 6) * 272), __builtin_bit_cast(bf16x8, pack8(s, 8, 1.0f)), y);
      }
    poff = (256 + rC * 16 + 4 * kq + (lane & 3)) * 128;
    ld8(pv, poff, flg);
    {
      const f32x16 s8 = MFMA(kf[8], q, nm);
      f32x16 s0;
      if (tmin <= 0) s0 = MFMA(kf[0], q, nm);
      else {
#pragma unroll
        for (int i = 0; i < 16; ++i) s0[i] = -__builtin_inff();
      }
      f32x16 p8, p0;
#pragma unroll
      for (int i = 0; i < 16; ++i) {
        const float p = __builtin_amdgcn_exp2f(dm[i] ? s8[i] : s0[i]);
        p8[i] = dm[i] ? p : 0.f;
        p0[i] = dm[i] ? 0.f : p;
      }
      y = MFMA(trfrag(va + (64) * 272, va + (64 + 2) * 272), __builtin_bit_cast(bf16x8, pack8(p8, 0, 1.0f)), y);
      y = MFMA(trfrag(va + (64 + 4) * 272, va + (64 + 6) * 272), __builtin_bit_cast(bf16x8, pack8(p8, 8, 1.0f)), y);
      if (tmin <= 0) {
        y = MFMA(trfrag(va, va + 2 * 272), __builtin_bit_cast(bf16x8, pack8(p0, 0, 1.0f)), y);
        y = MFMA(trfrag(va + 4 * 272, va + 6 * 272), __builtin_bit_cast(bf16x8, pack8(p0, 8, 1.0f)), y);
      }
    }
    st16<0>(l16, pack8(y, 0, __builtin_amdgcn_rcpf(y[8])), ws + WS_O + (u64)(T1 * 32 + 2 * j + wc) * 1024);
  }
  STAMP(6);
  FS(40);
  waitv<1>();
  TOUCH(pv);
  poll_finish(pv, true, poff, flg, tag0, tag1);
  FS(41);
  v4u lh[16];
  if (b == 0 && w == 7) {
#pragma unroll 1
    for (int k = 0; k < 4; ++k) {
      v2u v;
      const unsigned o = k * 8192 + l16 * 8;
      asm volatile("global_load_dwordx2 %0, %1, %2 sc1\n\ts_waitcnt vmcnt(0)" : "=v"(v) : "v"(o), "s"(flg) : "memory");
      poll_finish(v, true, o, flg, tag0, tag1);
    }
    if (l16 == 0) __hip_atomic_store((GAS u64*)(ws + WS_EPOCH), (((u64)ep1 << 32) | ep0) + 1u, __ATOMIC_RELAXED, __HIP_MEMORY_SCOPE_AGENT);
  }
  STAMP(7);
  f32x16 ga[2], sa[2];
#pragma unroll
  for (int i = 0; i < 16; ++i) { ga[0][i] = 0.f; ga[1][i] = 0.f; sa[0][i] = 0.f; sa[1][i] = 0.f; }
  unsigned da, oa, xoff;
  {
    const int lane = opq_tid() & 63, h = lane >> 5, l31 = lane & 31, q4 = (lane & 15) >> 2, p4 = lane & 3, blk = (lane >> 4) & 1;
    const unsigned colb = (32 * blk + 8 * p4) ^ ((q4 >> 1) << 6);
    da = L0 + (4 * h + q4) * 128 + colb;
    oa = L0 + (8 * h + q4) * 128 + colb;
    xoff = (64 * rt + 32 * tc + l31) * 1024 + (64 * ct + 16 * kq + 8 * h) * 2;
  }
  v4u of[8], xr;
  SCHED();
  FS(42);
  waitbar<4>();
  FS(43);
#pragma unroll
  for (int k = 0; k < 12; ++k) TOUCH(eh[k]);
  {
    const u64 lb = ws + WS_H + (u64)(TC * 112 + 48 + 16 * kq) * 1024;
    const u64 ob = ws + WS_O + (u64)(TC * 32 + 8 * kq) * 1024;
    bf16x8 f[LA + 1][2];
    unsigned a0, a1;
    auto rd = [&](auto I) {
      constexpr int i = I, st = i >> 2, ks = i & 3;
      if constexpr (ks == 0) {
        constexpr unsigned pk = CS[st][0] | (CS[st][1] << 8) | (CS[st][2] << 16) | (CS[st][3] << 24);
        unsigned k8 = 8 * kq; OPQ_S(k8);
        const unsigned base = ((pk >> k8) & 255u) * SLOT;
        a0 = (st < 7 ? da : oa) + base; a1 = a0 ^ 64u;
        TOUCH(a0); TOUCH(a1);
      }
      constexpr int d = st < 7 ? 1024 : 512;
      f[i % (LA + 1)][0] = trfrag(a0 + ks * 2048, a0 + ks * 2048 + d);
      f[i % (LA + 1)][1] = trfrag(a1 + ks * 2048, a1 + ks * 2048 + d);
    };
    sfor<LA>([&](auto I) { rd(I); });
    sfor<36>([&](auto I) {
      constexpr int i = I, st = i >> 2, ks = i & 3;
      if constexpr (ks == 0 && st >= 1) {
        FS(42 + 2 * st);
        if constexpr (st == 1) {
          waitbar<9>();
          if (threadIdx.x == 0) put_flag(flg + (u64)(512 + r * 16 + j) * 128, tag0, tag1);
        }
        if constexpr (st == 2) waitbar<10>();
        if constexpr (st == 3) {
          waitbar<9>();
          TOUCH(xr);
          poff = (512 + rC * 16 + 4 * kq + ((l16 >> 4) & 3)) * 128;
          ld8(pv, poff, flg);
        }
        if constexpr (st == 4) waitbar<10>();
        if constexpr (st == 5) {
          waitbar<5>();
          TOUCH(pv);
          poll_finish(pv, true, poff, flg, tag0, tag1);
        }
        if constexpr (st == 6) waitbar<8>();
        if constexpr (st == 7) waitbar<4>();
        if constexpr (st == 8) waitv<0>();
        FS(43 + 2 * st);
        if constexpr (st >= 3 && st < 7) {
#pragma unroll
          for (int k = 0; k < 4; ++k) TOUCH(lh[st >= 3 && st < 7 ? 4 * (st - 3) + k : 0]);
        }
        if constexpr (st >= 7) {
#pragma unroll
          for (int k = 0; k < 4; ++k) TOUCH(of[st >= 7 ? 4 * (st - 7) + k : 0]);
        }
      }
      if constexpr (i + LA < 36) rd(IC<(i + LA < 36 ? i + LA : 0)>{});
      SCHED();
      v4u au;
      if constexpr (i < 12) au = eh[i < 12 ? i : 0]; else if constexpr (i < 28) au = lh[i < 28 && i >= 12 ? i - 12 : 0]; else au = of[i >= 28 ? i - 28 : 0];
      const bf16x8 a = __builtin_bit_cast(bf16x8, au);
      if constexpr (i < 28) { ga[0] = MFMA(f[i % (LA + 1)][0], a, ga[0]); ga[1] = MFMA(f[i % (LA + 1)][1], a, ga[1]); }
      else { sa[0] = MFMA(f[i % (LA + 1)][0], a, sa[0]); sa[1] = MFMA(f[i % (LA + 1)][1], a, sa[1]); }
      SCHED();
      if constexpr (st < 6) FC(st < 6 ? st + 3 : 0, ks);
      if constexpr (st < 4) ld16<ks * 1024>(lh[i < 16 ? i : 0], l16, lb + st * 4096);
      if constexpr (st == 5 || st == 6) ld16<ks * 1024>(of[st == 5 || st == 6 ? i - 20 : 0], l16, ob + (st - 5) * 4096);
      if constexpr (i == 3) ld16p(xr, xoff, px);
    });
  }
  STAMP(8);
  FS(60);
  bar_l();
  FS(61);
  {
    unsigned rw = L0 + tc * 32768 + kq * 2048 + l16, rw2 = rw + 65536;
    TOUCH(rw); TOUCH(rw2);
#pragma unroll
    for (int d = 0; d < 4; ++d) {
#pragma unroll
      for (int k = 0; k < 2; ++k) {
        const int o = 8 * (d & 1) + 4 * k;
        *(LAS f32x4*)(rw + d * 8192 + k * 1024) = (f32x4){ga[d >> 1][o], ga[d >> 1][o + 1], ga[d >> 1][o + 2], ga[d >> 1][o + 3]};
        *(LAS f32x4*)(rw2 + d * 8192 + k * 1024) = (f32x4){sa[d >> 1][o], sa[d >> 1][o + 1], sa[d >> 1][o + 2], sa[d >> 1][o + 3]};
      }
    }
    bar_l();
    FS(62);
    unsigned rr = L0 + tc * 32768 + kq * 8192 + l16, rr2 = rr + 65536;
    TOUCH(rr); TOUCH(rr2);
    f32x4 g2[2], s2[2];
#pragma unroll
    for (int k = 0; k < 2; ++k) {
      g2[k] = *(LAS f32x4*)(rr + k * 1024);
      s2[k] = *(LAS f32x4*)(rr2 + k * 1024);
#pragma unroll
      for (int s = 1; s < 4; ++s) {
        g2[k] += *(LAS f32x4*)(rr + s * 2048 + k * 1024);
        s2[k] += *(LAS f32x4*)(rr2 + s * 2048 + k * 1024);
      }
    }
    float yv[8];
#pragma unroll
    for (int i = 0; i < 4; ++i) {
      const float a = s2[0][i] * g2[0][i] * __builtin_amdgcn_rcpf(1.0f + __builtin_amdgcn_exp2f(-1.44269504f * g2[0][i]));
      const float c = s2[1][i] * g2[1][i] * __builtin_amdgcn_rcpf(1.0f + __builtin_amdgcn_exp2f(-1.44269504f * g2[1][i]));
      const auto sw = __builtin_amdgcn_permlane32_swap(__builtin_bit_cast(unsigned, a), __builtin_bit_cast(unsigned, c), false, false);
      const unsigned sw0 = sw[0], sw1 = sw[1];
      yv[i] = __builtin_bit_cast(float, sw0);
      yv[4 + i] = __builtin_bit_cast(float, sw1);
    }
    v4u o;
#pragma unroll
    for (int i = 0; i < 4; ++i) {
      const unsigned xi = xr[i];
      o[i] = pk2(__builtin_bit_cast(float, xi << 16) + yv[2 * i], __builtin_bit_cast(float, xi & 0xffff0000u) + yv[2 * i + 1]);
    }
    *(GAS v4u*)(pout + xoff) = o;
  }
  STAMP(9);
  FS(63);
  asm volatile(".Lfb_end:");
}

extern "C" void kernel_launch(void* const* d_in, const int* in_sizes, int n_in, void* d_out, int out_size, void* d_ws, size_t ws_size,
                              hipStream_t stream) {
  fused_block<<<256, 512, 0, stream>>>((const char*)d_in[0], (const char*)d_in[1], (const char*)d_in[2], (const char*)d_in[4],
                                       (const char*)d_in[5], (const char*)d_in[6], (char*)d_out, (char*)d_ws);
}
```

```cpp
#include <hip/hip_runtime.h>
#include <hip/hip_bf16.h>
#include <utility>

typedef unsigned v4u __attribute__((ext_vector_type(4)));
typedef unsigned v2u __attribute__((ext_vector_type(2)));
typedef float f32x2 __attribute__((ext_vector_type(2)));
typedef float f32x4 __attribute__((ext_vector_type(4)));
typedef float f32x16 __attribute__((ext_vector_type(16)));
typedef __bf16 bf16x2 __attribute__((ext_vector_type(2)));
typedef __bf16 bf16x8 __attribute__((ext_vector_type(8)));
typedef short s16x4 __attribute__((ext_vector_type(4)));
typedef unsigned long long u64;
#define LAS __attribute__((address_space(3)))
#define GAS __attribute__((address_space(1)))
#define DI __device__ __forceinline__

constexpr u64 WS_EPOCH = 0;
constexpr u64 WS_FLAG = 4096;
constexpr u64 WS_ST = 256u << 10;
constexpr u64 WS_S = 512u << 10;
constexpr u64 WS_QKV = 1u << 20;
constexpr u64 WS_H = WS_QKV + (6u << 20);
constexpr u64 WS_STAMP = 32u << 20;
enum { FL0 = 0, FL1A = 256, FL1B = 512, FL2 = 768, FL3 = 1024 };
constexpr int SLOT = 8192;
constexpr int L_SCR = 19 * SLOT;
constexpr int L_V = 5 * SLOT;
constexpr int LDS_BYTES = 20 * SLOT;

#define MM_OP "v_mfma_f32_32x32x16_bf16 "
#define SCHED() __builtin_amdgcn_sched_barrier(0)
#define TOUCH(x) asm volatile("" : "+v"(x))
#define PIN(x) asm volatile("" ::"v"(x))
#define OPQ_S(x) asm volatile("" : "+s"(x))
#define BF8(x) __builtin_bit_cast(bf16x8, (x))

template <int LIMM> DI void dma_m0(unsigned lbase) { asm volatile("s_add_u32 m0, %0, %1" ::"s"(lbase), "n"(LIMM) : "memory", "scc"); }
DI void dma_go(unsigned voff, u64 sbase) { asm volatile("global_load_lds_dwordx4 %0, %1" ::"v"(voff), "s"(sbase) : "memory"); }
template <int D> DI void dma_go_i(unsigned& voff, u64 sbase) {
  asm volatile("v_add_u32 %0, %2, %0\n\tglobal_load_lds_dwordx4 %0, %1" : "+v"(voff) : "s"(sbase), "n"(D) : "memory");
}
DI void dma_go_s(unsigned& voff, u64 sbase, unsigned d) {
  asm volatile("v_add_u32 %0, %2, %0\n\tglobal_load_lds_dwordx4 %0, %1" : "+v"(voff) : "s"(sbase), "s"(d) : "memory");
}
DI void touch4(u64 a, unsigned l) { asm volatile("s_mov_b32 m0, %1\n\ts_nop 0\n\tglobal_load_lds_dword %0, off" ::"v"(a), "s"(l) : "memory"); }
DI void nop1() { asm volatile("s_nop 0" ::: "memory"); }
template <int N> DI void waitbar() { asm volatile("s_waitcnt vmcnt(%0) lgkmcnt(0)\n\ts_barrier" ::"n"(N) : "memory"); }
DI void bar_l() { asm volatile("s_waitcnt lgkmcnt(0)\n\ts_barrier" ::: "memory"); }
template <int N> DI void waitv() { asm volatile("s_waitcnt vmcnt(%0)" ::"n"(N) : "memory"); }
template <int OFF> DI void ld16(v4u& d, unsigned voff, u64 sbase) {
  asm volatile("global_load_dwordx4 %0, %1, %2 offset:%3 sc1" : "=v"(d) : "v"(voff), "s"(sbase), "n"(OFF) : "memory");
}
DI void ld16p(v4u& d, unsigned voff, u64 sbase) {
  asm volatile("global_load_dwordx4 %0, %1, %2" : "=v"(d) : "v"(voff), "s"(sbase) : "memory");
}
DI void ld8(u64& d, unsigned voff, u64 sbase) {
  asm volatile("global_load_dwordx2 %0, %1, %2 sc1" : "=v"(d) : "v"(voff), "s"(sbase) : "memory");
}
template <int OFF> DI void st16(unsigned voff, v4u d, u64 sbase) {
  asm volatile("global_store_dwordx4 %0, %1, %2 offset:%3 sc1\n\ts_nop 1" ::"v"(voff), "v"(d), "s"(sbase), "n"(OFF) : "memory");
}
DI void st4(unsigned voff, unsigned d, u64 sbase) { asm volatile("global_store_dword %0, %1, %2 sc1" ::"v"(voff), "v"(d), "s"(sbase) : "memory"); }
DI void st8(unsigned voff, u64 d, u64 sbase) { asm volatile("global_store_dwordx2 %0, %1, %2 sc1" ::"v"(voff), "v"(d), "s"(sbase) : "memory"); }

DI void mm_vva(f32x16& d, bf16x8 a, v4u b) { asm volatile(MM_OP "%0, %1, %2, %0" : "+v"(d) : "v"(a), "a"(b)); }
DI void mm_vva0(f32x16& d, bf16x8 a, v4u b) { asm volatile(MM_OP "%0, %1, %2, 0" : "=&v"(d) : "v"(a), "a"(b)); }
DI void mm_avv(f32x16& d, v4u a, v4u b) { asm volatile("s_nop 1\n\t" MM_OP "%0, %1, %2, %0" : "+a"(d) : "v"(a), "v"(b)); }
DI void mm_avv_nn(f32x16& d, v4u a, v4u b) { asm volatile(MM_OP "%0, %1, %2, %0" : "+a"(d) : "v"(a), "v"(b)); }
DI void mm_avv0(f32x16& d, v4u a, v4u b) { asm volatile("s_nop 1\n\t" MM_OP "%0, %1, %2, 0" : "=&a"(d) : "v"(a), "v"(b)); }
DI void mm_vvv(f32x16& d, v4u a, v4u b) { asm volatile(MM_OP "%0, %1, %2, %0" : "+v"(d) : "v"(a), "v"(b)); }
DI void mm_s0(f32x16& d, v4u k, v4u q) { asm volatile(MM_OP "%0, %1, %2, 0" : "=&v"(d) : "a"(k), "a"(q)); }
DI void mm_sc(f32x16& d, v4u k, v4u q, const f32x16& c) { asm volatile(MM_OP "%0, %1, %2, %3" : "=&v"(d) : "a"(k), "a"(q), "v"(c)); }
template <int N> DI void nops() { asm volatile("s_nop %0" ::"n"(N - 1)); }
template <int OFF> DI void ld16a(v4u& d, unsigned voff, u64 sbase) {
  asm volatile("global_load_dwordx4 %0, %1, %2 offset:%3 sc1" : "=a"(d) : "v"(voff), "s"(sbase), "n"(OFF) : "memory");
}
template <int OFF> DI void lds16a(v4u& d, unsigned a) { asm volatile("ds_read_b128 %0, %1 offset:%2" : "=a"(d) : "v"(a), "n"(OFF) : "memory"); }
template <int OFF> DI void sts16a(unsigned a, f32x4 d) { *(LAS f32x4*)(size_t)(a + OFF) = d; }
#define V4(x) __builtin_bit_cast(v4u, (x))
DI bf16x8 trfrag(unsigned a0, unsigned a1) {
  const s16x4 lo = __builtin_amdgcn_ds_read_tr16_b64_v4i16((LAS s16x4*)(size_t)a0);
  const s16x4 hi = __builtin_amdgcn_ds_read_tr16_b64_v4i16((LAS s16x4*)(size_t)a1);
  return __builtin_bit_cast(bf16x8, __builtin_shufflevector(lo, hi, 0, 1, 2, 3, 4, 5, 6, 7));
}
DI bf16x8 ldsfrag(unsigned a) { return *(LAS bf16x8*)(size_t)a; }
DI unsigned pk2(f32x2 v) { return __builtin_bit_cast(unsigned, __builtin_convertvector(v, bf16x2)); }
DI unsigned pk2(float a, float b) { return pk2((f32x2){a, b}); }
DI float bfr(float a) { return __builtin_bit_cast(float, pk2(a, a) & 0xffff0000u); }
DI float dot1(bf16x8 f, float acc) {
  const v4u u = __builtin_bit_cast(v4u, f);
  const bf16x2 one = {(__bf16)1.0f, (__bf16)1.0f};
#pragma unroll
  for (int i = 0; i < 4; ++i) { const unsigned e = u[i]; acc = __builtin_amdgcn_fdot2_f32_bf16(__builtin_bit_cast(bf16x2, e), one, acc, false); }
  return acc;
}
DI float halfsum(float v) {
  const unsigned u = __builtin_bit_cast(unsigned, v);
  const auto r = __builtin_amdgcn_permlane32_swap(u, u, false, false);
  const unsigned r0 = r[0], r1 = r[1];
  return __builtin_bit_cast(float, r0) + __builtin_bit_cast(float, r1);
}
DI float sum16x(float v) {
  const unsigned u = __builtin_bit_cast(unsigned, v);
  const auto r = __builtin_amdgcn_permlane16_swap(u, u, false, false);
  const unsigned r0 = r[0], r1 = r[1];
  return __builtin_bit_cast(float, r0) + __builtin_bit_cast(float, r1);
}
template <int CTRL> DI float dppadd(float v) {
  return v + __builtin_bit_cast(float, __builtin_amdgcn_update_dpp(0, __builtin_bit_cast(int, v), CTRL, 0xf, 0xf, true));
}
DI float max3(float a, float b, float c) { return __builtin_fmaxf(__builtin_fmaxf(a, b), c); }
DI f32x2 pr(const f32x16& a, int o) { return (f32x2){a[o], a[o + 1]}; }
DI v4u pack8(const f32x16& a, int o, float s) { return (v4u){pk2(pr(a, o) * s), pk2(pr(a, o + 2) * s), pk2(pr(a, o + 4) * s), pk2(pr(a, o + 6) * s)}; }
DI v4u pack8(const f32x16& a, int o) { return (v4u){pk2(pr(a, o)), pk2(pr(a, o + 2)), pk2(pr(a, o + 4)), pk2(pr(a, o + 6))}; }
DI float smul(float a, float b) { float d; asm("v_mul_f32 %0, %1, %2" : "=v"(d) : "v"(a), "v"(b)); return d; }
DI float sadd(float a, float b) { float d; asm("v_add_f32 %0, %1, %2" : "=v"(d) : "v"(a), "v"(b)); return d; }
DI float sfma(float a, float b, float c) { float d; asm("v_fma_f32 %0, %1, %2, %3" : "=v"(d) : "v"(a), "v"(b), "v"(c)); return d; }
DI f32x2 mul2(f32x2 a, f32x2 b) { return (f32x2){smul(a.x, b.x), smul(a.y, b.y)}; }
DI f32x2 add2(f32x2 a, f32x2 b) { return (f32x2){sadd(a.x, b.x), sadd(a.y, b.y)}; }
DI f32x2 fma2(f32x2 a, f32x2 b, f32x2 c) { return (f32x2){sfma(a.x, b.x, c.x), sfma(a.y, b.y, c.y)}; }
struct GK { float k1, k2, d2; };
DI GK gk_make(float rs) { return GK{-2.3022082f * rs, -0.10294324f * rs * rs * rs, __builtin_amdgcn_rcpf(rs * rs)}; }
DI f32x2 geglu_a(f32x2 a, const GK& k) {
  const f32x2 z = a * __builtin_elementwise_fma(a * a, (f32x2){k.k2, k.k2}, (f32x2){k.k1, k.k1});
  return (f32x2){__builtin_amdgcn_exp2f(z.x), __builtin_amdgcn_exp2f(z.y)};
}
DI unsigned geglu_b(f32x2 e, f32x2 a, f32x2 b, const GK& k) {
  const f32x2 d = __builtin_elementwise_fma(e, (f32x2){k.d2, k.d2}, (f32x2){k.d2, k.d2});
  return pk2((a * b) * (f32x2){__builtin_amdgcn_rcpf(d.x), __builtin_amdgcn_rcpf(d.y)});
}
DI unsigned hilo(float s) {
  const float hi = bfr(s);
  return pk2(hi, s - hi);
}
DI void poll_finish(u64 v, unsigned voff, u64 sbase, u64 tag) {
  if (__builtin_amdgcn_ballot_w64(v != tag) == 0ull) return;
#pragma unroll 1
  for (unsigned spins = 0; spins < (1u << 14); ++spins) {
    __builtin_amdgcn_s_sleep(1);
    asm volatile("global_load_dwordx2 %0, %1, %2 sc1\n\ts_waitcnt vmcnt(0)" : "=v"(v) : "v"(voff), "s"(sbase) : "memory");
    if (__builtin_amdgcn_ballot_w64(v != tag) == 0ull) break;
  }
}
template <int I> using IC = std::integral_constant<int, I>;
template <class F, int... Is> DI void sfor_impl(F&& f, std::integer_sequence<int, Is...>) { (f(IC<Is>{}), ...); }
template <int N, class F> DI void sfor(F&& f) { sfor_impl(f, std::make_integer_sequence<int, N>{}); }
constexpr int TS[8] = {15, 17, 10, 12, 0, 2, 5, 7};
constexpr int CS[9][4] = {{14, 4, 9, 19}, {15, 16, 17, 18}, {10, 11, 12, 13}, {0, 1, 2, 3}, {14, 4, 9, 19}, {15, 16, 17, 18}, {10, 11, 12, 13}, {0, 1, 2, 3}, {5, 6, 7, 8}};
constexpr unsigned short POST1[32][4] = {{0x120, 0x121, 0x122, 0}, {0x123, 0x124, 0x126, 0x128}, {0x125, 0x127, 0x129, 0}, {0x130, 0x131, 0x132, 0}, {0x133, 0x134, 0x136, 0}, {0x138, 0x135, 0, 0}, {0x137, 0x139, 0, 0}, {0x140, 0x141, 0x142, 0}, {0x143, 0x144, 0x146, 0}, {0x148, 0x145, 0, 0}, {0x147, 0x149, 0, 0}, {0x150, 0x151, 0x152, 0}, {0x153, 0x154, 0x156, 0}, {0x158, 0x155, 0, 0}, {0x157, 0x159, 0, 0}, {0x160, 0x161, 0x162, 0}, {0x163, 0x164, 0x166, 0}, {0x168, 0x165, 0, 0}, {0x167, 0x169, 0, 0}, {0x170, 0x171, 0x172, 0}, {0x173, 0x174, 0x176, 0}, {0x178, 0x175, 0, 0}, {0x177, 0x179, 0, 0}, {0x200, 0x201, 0, 0}, {0x202, 0x203, 0, 0}, {0x210, 0x211, 0, 0}, {0x212, 0x213, 0, 0}, {0x220, 0x221, 0x222, 0}, {0x223, 0x230, 0x231, 0}, {0x232, 0x233, 0x240, 0}, {0x241, 0x242, 0x243, 0}, {0x250, 0x251, 0x252, 0x253}};
constexpr unsigned short POST2[32][4] = {{0, 0, 0, 0}, {0, 0, 0, 0}, {0, 0, 0, 0}, {0, 0, 0, 0}, {0, 0, 0, 0}, {0, 0, 0, 0}, {0, 0, 0, 0}, {0x260, 0x300, 0, 0}, {0x261, 0x301, 0, 0}, {0x262, 0x302, 0, 0}, {0x263, 0x303, 0, 0}, {0x270, 0x304, 0, 0}, {0x271, 0x305, 0, 0}, {0x272, 0x306, 0, 0}, {0x273, 0x307, 0, 0}, {0x310, 0, 0, 0}, {0x311, 0, 0, 0}, {0x312, 0, 0, 0}, {0x313, 0, 0, 0}, {0x314, 0, 0, 0}, {0x315, 0, 0, 0}, {0x316, 0, 0, 0}, {0x317, 0, 0, 0}, {0x320, 0x321, 0, 0}, {0x900, 0x901, 0, 0}, {0x322, 0x323, 0, 0}, {0x324, 0x325, 0, 0}, {0x326, 0, 0, 0}, {0x327, 0, 0, 0}, {0x700, 0x701, 0x702, 0}, {0x703, 0x704, 0x705, 0}, {0x600, 0x601, 0x602, 0}};
constexpr unsigned short POSTG[8][3] = {{0x603, 0x604, 0x605}, {0x606, 0x607, 0}, {0x608, 0x609, 0}, {0x60a, 0x60b, 0}, {0, 0, 0}, {0, 0, 0}, {0, 0, 0}, {0, 0, 0}};
constexpr unsigned short POSTM[9][2] = {{0x400, 0}, {0x401, 0}, {0x402, 0x403}, {0x404, 0x405}, {0x406, 0x407}, {0x408, 0}, {0x409, 0x40a}, {0x40b, 0x40c}, {0x40d, 0x40e}};
constexpr unsigned short POSTA[10][6] = {{0x40f, 0x410, 0x411, 0x412, 0x413, 0x414}, {0x415, 0x416, 0x417, 0x330, 0x331, 0x332}, {0x333, 0x334, 0, 0, 0, 0}, {0x335, 0x336, 0x337, 0x340, 0x341, 0x342}, {0x343, 0x344, 0x345, 0x346, 0x347, 0x350}, {0x351, 0x352, 0x353, 0x354, 0x355, 0x356}, {0x357, 0x360, 0x361, 0x362, 0x363, 0x364}, {0x365, 0x366, 0x367, 0x500, 0x501, 0x502}, {0x503, 0x504, 0x505, 0x506, 0x507, 0x508}, {0x509, 0x50a, 0, 0, 0, 0}};
constexpr unsigned short POSTC[28][4] = {{0x50b, 0x380, 0, 0}, {0x50c, 0x381, 0x50d, 0}, {0x50e, 0x382, 0, 0}, {0x50f, 0x383, 0x510, 0}, {0x511, 0x384, 0, 0}, {0x512, 0x385, 0x513, 0}, {0x514, 0x386, 0, 0}, {0x515, 0x387, 0x516, 0}, {0x517, 0x518, 0, 0}, {0x519, 0, 0, 0}, {0x51a, 0, 0, 0}, {0x51b, 0, 0, 0}, {0x51c, 0, 0, 0}, {0x51d, 0, 0, 0}, {0x51e, 0, 0, 0}, {0x51f, 0, 0, 0}, {0x370, 0x371, 0, 0}, {0x372, 0x373, 0, 0}, {0x374, 0x375, 0, 0}, {0x376, 0x377, 0, 0}, {0, 0, 0, 0}, {0, 0, 0, 0}, {0, 0, 0, 0}, {0, 0, 0, 0}, {0x800, 0x801, 0x802, 0x803}, {0x804, 0x805, 0x806, 0x807}, {0x808, 0x809, 0x80a, 0x80b}, {0x80c, 0x80d, 0x80e, 0x80f}};
constexpr int WB[8] = {11, 11, 10, 11, 11, 10, 11, 11};
constexpr int WY[7] = {63, 63, 63, 62, 57, 57, 53};
constexpr int WO[4] = {12, 8, 4, 0};
constexpr int WL[28] = {63, 63, 63, 63, 63, 63, 63, 63, 63, 63, 63, 63, 34, 63, 32, 63, 28, 63, 26, 63, 23, 63, 19, 63, 15, 63, 19, 63};
constexpr int W_XS = 10, W_EP = 13, W_P0 = 13, W_Q0 = 6, W_Q1 = 13, W_Q3 = 8, W_Q6 = 10, W_P1A = 6, W_P0B = 7, W_CS2 = 2, W_CS2C = 6, W_ST1A = 8, W_ST1C = 8, W_ST1B = 11, W_S2 = 16, W_V = 17, W_KQ = 4, W_ST2 = 8, W_P2 = 18, W_PK2 = 59, W_YA = 63, W_OST = 7, W_P3 = 8, W_G0 = 16;

DI int opq_tid() { int t = threadIdx.x; asm volatile("" : "+v"(t)); return t; }
#define STAMP(i)
#define FS(i)
#define FT(i)

template <bool typeC>
DI void block_body(u64 px, u64 pq, u64 po, u64 pg, u64 pu, u64 pd, u64 pout, u64 ws, unsigned L0) {
  const int w = __builtin_amdgcn_readfirstlane(threadIdx.x >> 6);
  const int b = blockIdx.x, xcd = b & 7, idx = b >> 3;
  const int j = 4 * (xcd & 3) + (idx & 3), r = 8 * (xcd >> 2) + (idx >> 2);
  const int rt = 4 * xcd + (idx & 3), ct = idx >> 2;
  const unsigned w2k = L0 + w * 2048;
  const int tp = w & 1, wc = w >> 1, kq = w;
  const unsigned l16 = (threadIdx.x & 63) * 16;
  const u64 flg = ws + WS_FLAG;
  STAMP(0);
  FS(0); FT(0);

  const int unit0 = typeC ? 3 * j - 24 : j;
  constexpr unsigned rs12 = typeC ? 3072 : 3584, cs12 = 64 * rs12;
#define ROT 1
  const int rot = (2 * (r & 3) + (r >> 3) + (idx & 2)) & 7, chf = (r >> 2) & 1, rotc = ROT ? (idx & 3) + ct : 0;
  unsigned vox[2], voq, vog, vo12, wsw;
  {
    const int lane = threadIdx.x & 63;
    const unsigned rowi = 16 * w + (lane >> 3) + 64 * rot;
    wsw = ((lane & 7) ^ (((lane >> 4) & 1) << 2)) * 16;
    vox[0] = (16 * w + (lane >> 3)) * 1024 + (((lane & 7) ^ (lane >> 4)) * 16) + rot * 128;
    vox[1] = (16 * w + 8 + (lane >> 3)) * 1024 + (((lane & 7) ^ (4 + (lane >> 4))) * 16) + rot * 128;
    voq = rowi * 3072 + wsw; vog = rowi * 3584 + wsw;
    vo12 = vog;
  }
  const u64 bx = px + (u64)r * 131072, bxh = bx + 65536;
  const u64 bu0 = pq + unit0 * 128, bu1 = typeC ? bu0 + 128 : pg + j * 128, bu2 = typeC ? bu0 + 256 : pu + j * 128;
  const u64 cu0 = bu0 + 8 * 3072, cu1 = bu1 + 8 * rs12, cu2 = bu2 + 8 * rs12;
  const u64 bg2 = pg + (12 + j) * 128, bu2b = pu + (12 + j) * 128, cg2 = bg2 + 8 * 3584, cu2b = bu2b + 8 * 3584;
  u64 bwd = pd + ct * 128, bwo = po + ct * 128;
  u64 cb[2];
  unsigned voc[8], cm0[2];
  unsigned rot3 = (unsigned)rotc % 3u;
  auto cblk = [&](int st, unsigned q) -> unsigned {
    unsigned rc = st < 3 ? rot3 : rotc; OPQ_S(rc);
    return st < 3 ? 3 * q + (st + rc >= 3 ? st + rc - 3 : st + rc) : (st < 7 ? 12 + 4 * q + ((st - 3 + rc) & 3) : 2 * q + ((st - 7 + rc) & 1));
  };
  auto step = [&](int a, unsigned d) -> unsigned { unsigned ro = rot; OPQ_S(ro); return a + ro == 8 ? d - 8 * d : d; };
  const int T1 = 4 * r + 2 * tp, head = 2 * j + wc;
  v4u vst[6], kf[10], qf[2];
  v4u eh[24], lh[32], of[16];
  u64 hb0;
  const u64 hbase = ws + WS_H + (u64)(2 * rt * 112) * 1024, qkvh = ws + WS_QKV + (u64)head * 65536;
  const u64 obase = ws + WS_QKV + (u64)(2 * rt + (kq & 1)) * 1024;
  unsigned lo4[4];
  const unsigned so16 = ((l16 & 496) << 1) | ((l16 >> 9) << 4);
  v4u sf[3];
  unsigned lt[2], kva, kvb;

  auto fill_a = [&](auto C) {
    constexpr int c = C, kind = c >> 8, a = (c >> 4) & 15, k = c & 15;
    if constexpr (kind == 1) dma_m0<((a % 3) * 5 + (k >> 1)) * SLOT + (k & 1) * 1024>(w2k);
    if constexpr (kind == 2) dma_m0<(TS[a & 7] + (k >> 1)) * SLOT + (k & 1) * 1024>(w2k);
    if constexpr (kind == 3) {
      if constexpr (k == 0) {
        constexpr unsigned pk = CS[a % 9][0] | (CS[a % 9][1] << 8) | (CS[a % 9][2] << 16) | (CS[a % 9][3] << 24);
        unsigned k8 = 8 * kq; OPQ_S(k8);
        cm0[a & 1] = L0 + ((pk >> k8) & 255u) * SLOT;
        cb[a & 1] = (a < 7 ? bwd : bwo) + (u64)cblk(a, kq) * 65536;
      }
      dma_m0<k * 1024>(cm0[a & 1]);
    }
  };
  auto fill_b = [&](auto C) {
    constexpr int c = C, kind = c >> 8, a = (c >> 4) & 15, k = c & 15, n = c & 255;
    if constexpr (kind == 1) {
      if constexpr (k < 2) { if constexpr (a > 0) dma_go_s(vox[k & 1], bx, step(a, 128)); else dma_go(vox[k & 1], bx); }
      if constexpr (k == 2 || k == 3) dma_go(vox[k & 1], bxh);
      if constexpr (k == 4) { if constexpr (a > 0) dma_go_s(voq, bu0, step(a, 64 * 3072)); else dma_go(voq, bu0); }
      if constexpr (k == 5) dma_go(voq, cu0);
      if constexpr (k == 6) { if constexpr (typeC) dma_go(voq, bu1); else if constexpr (a > 0) dma_go_s(vo12, bu1, step(a, cs12)); else dma_go(vo12, bu1); }
      if constexpr (k == 7) dma_go(typeC ? voq : vo12, cu1);
      if constexpr (k == 8) dma_go(typeC ? voq : vo12, bu2);
      if constexpr (k == 9) dma_go(typeC ? voq : vo12, cu2);
    }
    if constexpr (kind == 2) {
      if constexpr (k == 0) { if constexpr (a > 0) dma_go_s(vog, bg2, step(a, 64 * 3584)); else dma_go(vog, bg2); }
      if constexpr (k == 1) dma_go(vog, cg2);
      if constexpr (k == 2) dma_go(vog, bu2b);
      if constexpr (k == 3) dma_go(vog, cu2b);
    }
    if constexpr (kind == 3) dma_go(voc[k & 7], cb[a & 1]);
    if constexpr (kind == 4 || kind == 5) {
      if constexpr ((n & 7) == 0) hb0 = hbase + cblk((kind == 5 ? 3 : 0) + (n >> 3), kq) * 4096;
      if constexpr (kind == 4) ld16a<((n >> 1) & 3) * 1024>(eh[n % 24], lt[n & 1], hb0);
      else ld16a<((n >> 1) & 3) * 1024>(lh[n % 32], lt[n & 1], hb0);
    }
    if constexpr (kind == 6) {
      if constexpr (n < 10) ld16a<0>(kf[n % 10], (unsigned)max((int)kva + n * 1024, (int)(l16 & 512u)), qkvh + 32 * 65536);
      else ld16a<(n & 1) * 1024>(qf[n & 1], kva + 8192, qkvh);
    }
    if constexpr (kind == 8) {
      if constexpr (n == 0) { lo4[0] = l16; lo4[1] = l16 + 65536; lo4[2] = l16 + 131072; lo4[3] = l16 + 196608; }
      if constexpr ((n & 3) == 0) hb0 = obase + (2 * ((n >> 2) & 3) + ((rotc + (kq >> 1)) & 1)) * 262144;
      ld16a<0>(of[n % 16], lo4[n & 3], hb0);
    }
    if constexpr (kind == 9) ld16<(n & 1) * 1024>(sf[n & 1], so16, ws + WS_S + (u64)((j * 10 + 6 + wc * 2) * 1024));
    if constexpr (kind == 7) ld16<0>(vst[n % 6], (unsigned)max((int)kvb + n * 1024, (int)(l16 & 512u)), qkvh + 64 * 65536);
  };
  auto fill = [&](auto C) { if constexpr (C != 0) { fill_a(C); if constexpr ((C >> 8) <= 3) nop1(); fill_b(C); } };
  u64 ep;
  v4u xs[2];
  ld16p(xs[0], l16, px + (u64)(128 * r + 8 * j + 2 * w) * 1024);
  ld16p(xs[1], l16, px + (u64)(128 * r + 8 * j + 2 * w + 1) * 1024);
  fill(IC<0x100>{}); fill(IC<0x101>{}); fill(IC<0x102>{}); fill(IC<0x103>{}); fill(IC<0x104>{}); fill(IC<0x106>{}); fill(IC<0x108>{});
  fill(IC<0x105>{}); fill(IC<0x107>{}); fill(IC<0x109>{});
  FT(1);
  const int rC = rt >> 1;
  unsigned wa, xa[4], ca, va, vw, xoff, pof1a, pofw;
  int dl;
  {
    const int lane = threadIdx.x & 63, h = lane >> 5, l31 = lane & 31, q4 = (lane & 15) >> 2, p4 = lane & 3, blk = (lane >> 4) & 1;
    wa = L0 + 2 * SLOT + (8 * h + q4) * 128 + ((64 * wc + 32 * blk + 8 * p4) ^ ((q4 >> 1) << 6));
#pragma unroll
    for (int ks = 0; ks < 4; ++ks) { xa[ks] = L0 + tp * SLOT + l31 * 128 + (((2 * ks + h) ^ ((lane >> 1) & 7)) << 4); PIN(xa[ks]); }
#pragma unroll
    for (int n = 0; n < 8; ++n) { voc[n] = (8 * n + (lane >> 3)) * 1024 + wsw; PIN(voc[n]); }
    PIN(wa);
    ca = L0 + (4 * h + q4) * 128 + (((32 * blk + 8 * p4) ^ (64 * (kq >> 1))) ^ ((q4 >> 1) << 6));
    va = L0 + L_V + (2 * tp * 8 + h) * 272 + (blk ? 256 + 8 * (p4 & 1) : q4 * 64 + wc * 32 + 8 * p4);
    vw = L0 + L_V + (6 * tp * 8 + (l31 >> 2)) * 272 + (l31 & 3) * 64 + wc * 32 + h * 16;
    lt[0] = l16 + (kq & 1) * (112 * 1024); lt[1] = l16 + ((kq & 1) ^ 1) * (112 * 1024);
    kva = l16 + (T1 - 8) * 1024; kvb = l16 + (4 * r - 8 + 6 * tp) * 1024;
    xoff = (64 * rt + 32 * (kq & 1) + l31) * 1024 + (64 * ct + 32 * (kq >> 1) + 8 * h) * 2;
    dl = l31 - 4 * h;
    const int pi = (lane >> 1) & 7, g4 = j >> 1, uk = 8 + g4, uv = 16 + g4;
    const int jk = uk < 12 ? uk : 12 + (uk - 12) / 3, jv = 12 + (uv - 12) / 3;
    int fr = r, fj = g4;
    if (pi >= 1 && pi <= 3) { fr = r - 3 + pi; fj = jk; }
    if (pi >= 4 && pi <= 6) { fr = r - 6 + pi; fj = jv; }
    fr = fr < 0 ? 0 : fr;
    pof1a = (FL1A + fr * 16 + fj) * 128 + (2 * (j & 1) + (lane & 1)) * 8;
    pofw = rC * 2048 + (rt & 1) * 8 + (lane & 15) * 128 + (lane & 16);
    TOUCH(ca); TOUCH(va); TOUCH(vw); TOUCH(lt[0]); TOUCH(lt[1]); TOUCH(kva); TOUCH(kvb); TOUCH(xoff); TOUCH(dl); TOUCH(pof1a); TOUCH(pofw);
  }
  FT(2);
  if (w == 3) {
    const unsigned ln = l16 >> 4, k = ln < 5 ? ln : (ln < 10 ? ln - 5 : 0);
    u64 a = flg + (u64)(r * 16 + j) * 128 + k * 32768;
    if (ln >= 5) a = ws + WS_S + (u64)((j * 10 + (k < 3 ? 3 * chf + k : 6 + 2 * chf + k - 3)) * 1024 + rot * 128);
    if (ln >= 10) a = ws + WS_ST + (u64)(128 * r + 8 * j) * 8;
    touch4(a, L0 + L_SCR + 2048);
  }
  {
    auto both = [](f32x2 v, auto fn) { return (f32x2){fn(v.x), fn(v.y)}; };
    auto wsum = [&](f32x2 v) {
      v = both(v, [](float x) { return dppadd<0xB1>(x); }); v = both(v, [](float x) { return dppadd<0x4E>(x); });
      v = both(v, [](float x) { return dppadd<0x141>(x); }); v = both(v, [](float x) { return dppadd<0x140>(x); });
      v = both(v, [](float x) { return sum16x(x); });
      return both(v, [](float x) { return halfsum(x); });
    };
    waitv<W_XS>();
    TOUCH(xs[0]); TOUCH(xs[1]);
    f32x2 xv[8];
#pragma unroll
    for (int k = 0; k < 4; ++k) {
      const unsigned e = xs[0][k], e2 = xs[1][k];
      xv[2 * k] = (f32x2){__builtin_bit_cast(float, e << 16), __builtin_bit_cast(float, e2 << 16)};
      xv[2 * k + 1] = (f32x2){__builtin_bit_cast(float, e & 0xffff0000u), __builtin_bit_cast(float, e2 & 0xffff0000u)};
    }
    f32x2 ra = ((xv[0] + xv[1]) + (xv[2] + xv[3])) + ((xv[4] + xv[5]) + (xv[6] + xv[7]));
    PIN(ra);
    fill(IC<0x110>{}); fill(IC<0x111>{});
    const f32x2 mean = wsum(ra) * (1.0f / 512.0f);
    PIN(mean);
    fill(IC<0x112>{}); fill(IC<0x113>{});
#pragma unroll
    for (int k = 0; k < 8; ++k) xv[k] -= mean;
    ra = __builtin_elementwise_fma(xv[3], xv[3], __builtin_elementwise_fma(xv[2], xv[2], __builtin_elementwise_fma(xv[1], xv[1], xv[0] * xv[0]))) +
         __builtin_elementwise_fma(xv[7], xv[7], __builtin_elementwise_fma(xv[6], xv[6], __builtin_elementwise_fma(xv[5], xv[5], xv[4] * xv[4])));
    PIN(ra);
    fill(IC<0x114>{}); fill(IC<0x116>{});
    const f32x2 var = wsum(ra) * (1.0f / 512.0f);
    PIN(var);
    fill(IC<0x118>{}); fill(IC<0x115>{});
    const f32x2 rs = both(var, [](float v) { return __builtin_amdgcn_rcpf(bfr(__builtin_amdgcn_sqrtf(bfr(bfr(v) + 1e-5f)))); });
    PIN(rs);
    fill(IC<0x117>{}); fill(IC<0x119>{});
    st8(0u, (u64)pk2(-mean.x, -mean.x) | ((u64)__builtin_bit_cast(unsigned, (float)rs.x) << 32), ws + WS_ST + (u64)(128 * r + 8 * j + 2 * w) * 8);
    st8(8u, (u64)pk2(-mean.y, -mean.y) | ((u64)__builtin_bit_cast(unsigned, (float)rs.y) << 32), ws + WS_ST + (u64)(128 * r + 8 * j + 2 * w) * 8);
  }
  if (w == 0) {
    u64 pc;
    unsigned len;
    if constexpr (typeC) asm volatile("s_getpc_b64 %0\n.Lfb_here1:\n\ts_mov_b32 %1, .Lfb_end1-.Lfb_here1" : "=s"(pc), "=s"(len));
    else asm volatile("s_getpc_b64 %0\n.Lfb_here0:\n\ts_mov_b32 %1, .Lfb_end0-.Lfb_here0" : "=s"(pc), "=s"(len));
    if (idx * 1024 + 1024 <= len) { dma_m0<L_SCR>(L0); nop1(); dma_go(l16, (pc & ~(u64)127) + idx * 1024); }
    if (idx * 1024 + 33792 <= len) { dma_m0<L_SCR>(L0); nop1(); dma_go(l16, (pc & ~(u64)127) + idx * 1024 + 32768); }
  }
  ld8(ep, 0u, ws + WS_EPOCH);
  FT(3);
  u64 tag, eps;
  const u64 myf = flg + (u64)(r * 16 + j) * 128;
  auto put_flag = [&](int fl) { if (w == 0) st8(fl * 128, tag, myf); };

  v4u xf[32][2];
  f32x16 acc[3][2], ac2[2][2];
  bf16x8 f[2][3];
  u64 pv, stv[2];
  unsigned poff;
  v4u mf[2];
  auto rdx = [&](auto I, auto T) {
    constexpr int i = I, t = T, s = i >> 2, ks = i & 3;
    if constexpr (s > 0 && t == 0) { xa[ks] += ((s % 3) - ((s - 1) % 3)) * 5 * SLOT; TOUCH(xa[ks]); }
    lds16a<t * 4096>(xf[i][t], xa[ks]);
  };
  auto rdw = [&](auto I, auto U) {
    constexpr int i = I, u = U, s = (i >> 2) & 7, ks = i & 3;
    if constexpr (u == 0 && ks == 0 && i > 0) {
      constexpr int cur = i < 32 ? (s % 3) * 5 + 2 : TS[s], prev = i <= 32 ? (((i >> 2) - 1) % 3) * 5 + 2 : TS[(s + 7) & 7];
      wa += (cur - prev) * SLOT; TOUCH(wa);
    }
    if constexpr (u < (i < 32 ? 3 : 2)) f[i & 1][u] = trfrag(wa + u * SLOT + ks * 2048, wa + u * SLOT + ks * 2048 + 512);
  };
  SCHED();
  FS(1);
  waitbar<WB[0]>();
  FS(2);
  STAMP(1);
  u64 qb0 = ws + WS_QKV + (u64)((4 * unit0 + 2 * wc) * 64 + T1) * 1024, hb1 = ws + WS_H + (u64)(T1 * 112 + 4 * j + 2 * wc) * 1024;
  u64 csb = ws + WS_S + (u64)((j * 10 + w) * 1024 + rot * 4);
  bf16x8 cf[4];
  float cd0, cd1;
  float rstd[2], rq[2];
  GK gk[2];
  auto csum_rd = [&](int nu, int sl) {
    const unsigned a = (wa ^ (chf != wc ? 64u : 0u)) + ((w < nu ? w : 0) + sl) * SLOT;
#pragma unroll
    for (int k = 0; k < 4; ++k) cf[k] = trfrag(a + k * 2048, a + k * 2048 + 512);
  };
  auto csum_dot = [&]() {
    const float d0 = dot1(cf[0], 0.f), d1 = dot1(cf[1], 0.f), d2 = dot1(cf[2], 0.f), d3 = dot1(cf[3], 0.f);
    cd0 = d0 + d1; cd1 = d2 + d3; PIN(cd0); PIN(cd1);
  };
  auto csum_put = [&](int tile0, int nu) {
    if (w < nu) st4((l16 & 496) << 1, hilo(halfsum(cd0 + cd1)), csb + (tile0 + nu * chf) * 1024);
  };
  auto gap = [&](auto G) {
    constexpr int g = G;
    const unsigned fl8 = l16 * 8;
    const u64 fa = flg;
    if constexpr (g == 24) {
      waitv<W_EP>();
      TOUCH(ep);
      const unsigned e0 = __builtin_amdgcn_readfirstlane((unsigned)ep), e1 = __builtin_amdgcn_readfirstlane((unsigned)(ep >> 32));
      eps = (u64)e0 | ((u64)e1 << 32);
      tag = (u64)(e0 * 0x9E3779B1u + 0x7F4A7C15u) | ((u64)((e1 ^ e0) * 0x85EBCA77u + 0x165667B1u) << 32);
    }
    if constexpr (g == 11) csum_rd(3, 0);
    if constexpr (g == 41) { OPQ_S(qb0); OPQ_S(hb1); OPQ_S(bwd); OPQ_S(bwo); OPQ_S(rot3); }
    if constexpr (g == 186) csum_rd(2, 8);
    if constexpr (g == 187 || g == 188) {
      constexpr int t = g & 1;
      const unsigned mm = (unsigned)stv[t];
      mf[t] = (v4u){mm, mm, mm, mm}; PIN(mf[t]);
      rstd[t] = __builtin_bit_cast(float, (unsigned)(stv[t] >> 32));
      rq[t] = rstd[t] * (unit0 < 8 ? 0.36067376f : 1.0f); PIN(rq[t]);
    }
    if constexpr (g == 189 || g == 190) { gk[g & 1] = gk_make(rstd[g & 1]); PIN(gk[g & 1].k1); PIN(gk[g & 1].k2); PIN(gk[g & 1].d2); }
    if constexpr (g == 23) csum_put(0, 3);
    if constexpr (g == 179) {
      if (b == 0 && w == 3) {
        u64 ev[4];
        asm volatile("global_load_dwordx2 %0, %4, %5 sc1\n\tglobal_load_dwordx2 %1, %4, %6 sc1\n\tglobal_load_dwordx2 %2, %4, %7 sc1\n\t"
                     "global_load_dwordx2 %3, %4, %8 sc1\n\ts_waitcnt vmcnt(0)"
                     : "=&v"(ev[0]), "=&v"(ev[1]), "=&v"(ev[2]), "=&v"(ev[3]) : "v"(fl8), "s"(fa), "s"(fa + 8192), "s"(fa + 16384), "s"(fa + 24576) : "memory");
#pragma unroll
        for (int k = 0; k < 4; ++k) poll_finish(ev[k], k * 8192 + l16 * 8, flg, tag);
        st8(0u, eps + 1u, ws + WS_EPOCH);
      }
    }
    if constexpr (g == 65) {
      const unsigned ln = l16 >> 4, line = b * 72 + w * 18 + (ln < 18 ? ln : 17);
      const u64 a = line < 14336 ? pd + (u64)line * 128 : po + (u64)(line - 14336) * 128;
      touch4(a, L0 + L_SCR + 1024);
    }
  };
  rdx(IC<0>{}, IC<0>{}); rdx(IC<0>{}, IC<1>{}); rdw(IC<0>{}, IC<0>{}); rdw(IC<0>{}, IC<1>{}); rdw(IC<0>{}, IC<2>{});
  sfor<32>([&](auto I) {
    constexpr int i = I;
    sfor<6>([&](auto M) {
      constexpr int m = M, u = m >> 1, t = m & 1;
      SCHED();
      if constexpr (i == 0) mm_vva0(acc[u][t], f[0][u], xf[0][t]); else mm_vva(acc[u][t], f[i & 1][u], xf[i][t]);
      SCHED();
      if constexpr (m == 0 && (i & 3) == 3) {
        constexpr int s = (i + 1) >> 2;
        if constexpr (s == 1) csum_dot();
        FS(1 + 2 * s);
        if constexpr (s < 8) waitbar<WB[s & 7]>(); else waitbar<W_Q0>();
        FS(2 + 2 * s);
        if constexpr (s == 3) put_flag(FL0);
        if constexpr (s == 8) { TOUCH(stv[0]); TOUCH(stv[1]); TOUCH(sf[0]); TOUCH(sf[1]); TOUCH(sf[2]); }
      }
      if constexpr (m >= 2) fill_b(IC<POST1[i][(m - 2) & 3]>{});
      if constexpr (m < 2 && i < 31) rdx(IC<(i + 1) & 31>{}, IC<m>{});
      if constexpr (m >= 2 && m <= 4) rdw(IC<i + 1>{}, IC<(m - 2) & 3>{});
      gap(IC<6 * i + m>{});
      if constexpr (i == 26 && m == 0) { waitv<W_P0>(); TOUCH(pv); poll_finish(pv, poff, flg, tag); }
      if constexpr (i == 20 && m == 0) {
        poff = l16 < 256 ? r * 2048 + l16 * 8 : ((l16 & 240) * 128 + j * 128);
        ld8(pv, poff, flg);
      }
      if constexpr (m >= 1 && m <= 4) fill_a(IC<POST1[i][(m - 1) & 3]>{});
    });
    if constexpr (i == 26) {
      ld8(stv[0], (l16 & 496) >> 1, ws + WS_ST + (u64)(128 * r + 64 * tp) * 8);
      ld8(stv[1], (l16 & 496) >> 1, ws + WS_ST + (u64)(128 * r + 64 * tp + 32) * 8);
    }
    if constexpr (i >= 26 && i <= 28) ld16<(i >= 26 && i <= 28 ? i - 26 : 0) * 1024>(sf[i >= 26 && i <= 28 ? i - 26 : 0], so16, ws + WS_S + (u64)((j * 10 + wc * 3) * 1024));
  });
  SCHED();
  csum_dot();
  SCHED();
#pragma unroll
  for (int u = 0; u < 3; ++u) { mm_vvv(acc[u][0], sf[u], mf[0]); mm_vvv(acc[u][1], sf[u], mf[1]); }
  STAMP(2);
  unsigned a0, a1;
  const unsigned lt2[2] = {l16, l16 + 112 * 1024};
  bf16x8 pk[3][8];
  unsigned pa0, pa1;
  auto park = [&](auto ST, auto KS) {
    constexpr int st = ST, ks = KS;
    if constexpr (ks == 0) {
      constexpr unsigned pkk = CS[st][0] | (CS[st][1] << 8) | (CS[st][2] << 16) | (CS[st][3] << 24);
      unsigned k8 = 8 * kq; OPQ_S(k8);
      pa0 = ca + ((pkk >> k8) & 255u) * SLOT; pa1 = pa0 ^ 64u;
      TOUCH(pa0); TOUCH(pa1);
    }
    pk[st][2 * ks] = trfrag(pa0 + ks * 2048, pa0 + ks * 2048 + 1024);
    pk[st][2 * ks + 1] = trfrag(pa1 + ks * 2048, pa1 + ks * 2048 + 1024);
  };
  {
    v4u hv;
    f32x2 gz[2], gw[2];
    auto gstep = [&](auto P, auto S) {
      constexpr int p = P, st = S;
      if constexpr (!typeC && p >= 0 && p < 16) {
        constexpr int t = p >> 3, e = 2 * (p & 7);
        const f32x2 a = pr(acc[1][t], e);
        f32x2& z = gz[p & 1];
        if constexpr (st == 0) z = mul2(a, a);
        if constexpr (st == 1) z = fma2(z, (f32x2){gk[t].k2, gk[t].k2}, (f32x2){gk[t].k1, gk[t].k1});
        if constexpr (st == 2) z = mul2(a, z);
        if constexpr (st == 3) z = (f32x2){__builtin_amdgcn_exp2f(z.x), __builtin_amdgcn_exp2f(z.y)};
        if constexpr (st == 4) { z = fma2(z, (f32x2){gk[t].d2, gk[t].d2}, (f32x2){gk[t].d2, gk[t].d2}); gw[p & 1] = mul2(a, pr(acc[2][t], e)); PIN(gw[p & 1].x); PIN(gw[p & 1].y); }
        if constexpr (st == 5) z = (f32x2){__builtin_amdgcn_rcpf(z.x), __builtin_amdgcn_rcpf(z.y)};
        if constexpr (st == 6) z = mul2(gw[p & 1], z);
        if constexpr (st < 7) { PIN(z.x); PIN(z.y); }
        if constexpr (st == 7) {
          hv[p & 3] = pk2(z); PIN(hv[p & 3]);
          if constexpr ((p & 3) == 3) st16<(e >> 3) * 1024>(lt2[t], hv, hb1);
        }
      }
    };
    fill_a(IC<POST2[0][0]>{});
    sfor<32>([&](auto I) {
      constexpr int i = I;
      sfor<4>([&](auto M) {
        constexpr int m = M, u = m >> 1, t = m & 1;
        SCHED();
        if constexpr (i == 0) mm_vva0(ac2[u][t], f[0][u], xf[0][t]); else mm_vva(ac2[u][t], f[i & 1][u], xf[i][t]);
        SCHED();
        if constexpr (m == 0 && (i & 3) == 3) {
          constexpr int s = (i + 1) >> 2;
          FS(17 + 2 * s);
          if constexpr (s == 1) waitbar<W_Q1>();
          if constexpr (s == 3) waitbar<W_Q3>();
          if constexpr (s == 6) waitbar<W_Q6>();
          if constexpr (s == 7 && !typeC) { waitv<W_ST1B>(); st8(FL1B * 128 + w * 8, tag, myf); }
          if constexpr (s == 8) bar_l();
          FS(18 + 2 * s);
        }
        if constexpr (m == 0 && i == (typeC ? 10 : 8)) {
          waitv<(typeC ? W_CS2C + 12 : W_CS2 + 4)>();
          st8(FL0 * 128 + 32 + w * 8, tag, myf);
        }
        if constexpr (m == 0 && i == 11) { waitv<(typeC ? W_ST1C : W_ST1A)>(); st8(FL1A * 128 + w * 8, tag, myf); }
        if constexpr (m == 1 && i == 0) csum_put(6, 2);
        if constexpr (m == 0 && i == 18) {
          const int pi = (l16 >> 5) & 7;
          poff = (FL0 + (8 * (pi >> 2) + 4 * wc + (pi & 3)) * 16 + j) * 128 + 32 + (l16 & 16) / 2;
          ld8(pv, poff, flg);
        }
        if constexpr (m == 0 && i == 24) { waitv<W_P0B>(); TOUCH(pv); poll_finish(pv, poff, flg, tag); }
        if constexpr (m == 0 && i == 29) { waitv<W_P1A>(); TOUCH(pv); poll_finish(pv, poff, flg, tag); }
        fill_b(IC<POST2[i][m]>{});
        if constexpr (m == 3 && i == 24) { poff = pof1a; ld8(pv, poff, flg); }
        if constexpr ((m == 0 || m == 2) && i < 31) rdw(IC<33 + (i < 31 ? i : 0)>{}, IC<(m >> 1)>{});
        {
          constexpr int G = 4 * i + m - 2, p = G >> 1;
          if constexpr (G >= 0 && p < (typeC ? 12 : 4)) {
            constexpr int uu = (p >> 2) % 3, t2 = (p >> 1) & 1, o = 8 * (p & 1) + 4 * (G & 1);
            const float sc = uu == 0 ? rq[t2] : rstd[t2];
            const float z0 = smul(acc[uu][t2][o], sc), z1 = smul(acc[uu][t2][o + 1], sc), z2 = smul(acc[uu][t2][o + 2], sc), z3 = smul(acc[uu][t2][o + 3], sc);
            PIN(z0); PIN(z1); PIN(z2); PIN(z3);
            SCHED();
            hv[2 * (G & 1)] = pk2(z0, z1);
            hv[2 * (G & 1) + 1] = pk2(z2, z3);
            PIN(hv[2 * (G & 1)]); PIN(hv[2 * (G & 1) + 1]);
            if constexpr (G & 1) st16<t2 * 1024>(l16, hv, qb0 + (4 * uu + (p & 1)) * 65536);
          }
        }
        gstep(IC<i - 4>{}, M); gstep(IC<i - 5>{}, IC<m + 4>{});
        if constexpr (m < 3) fill_a(IC<POST2[i][(m + 1) & 3]>{}); else fill_a(IC<POST2[(i + 1) & 31][0] * (i < 31)>{});
      });
    });
  }
  STAMP(4);
  {
    FT(10);
    waitv<W_S2>();
    FT(11);
    TOUCH(sf[0]); TOUCH(sf[1]); PIN(mf[0]); PIN(mf[1]);
    SCHED();
    nops<2>();
#pragma unroll
    for (int u = 0; u < 2; ++u) { mm_vvv(ac2[u][0], sf[u], mf[0]); mm_vvv(ac2[u][1], sf[u], mf[1]); }
    const u64 hb = hb1 + 48 * 1024;
    v4u hv;
    f32x2 ez[4];
    poff = FL1B * 128 + 3 * kq * 128 + ((pofw & 384u) == 384u ? pofw & ~1920u : pofw & ~1536u);
    v4u hv0;
    nops<10>();
    sfor<8>([&](auto G) {
      constexpr int g = G, t = g >> 2, e = 8 * ((g >> 1) & 1);
      SCHED();
      fill_a(IC<POSTG[g][0]>{});
      if constexpr (g >= 4) park(IC<0>{}, IC<g & 3>{});
      if constexpr (!(g & 1)) {
#pragma unroll
        for (int k = 0; k < 4; ++k) { ez[k] = geglu_a(pr(ac2[0][t], e + 2 * k), gk[t]); PIN(ez[k]); }
      } else {
#pragma unroll
        for (int k = 0; k < 4; ++k) { hv[k] = geglu_b(ez[k], pr(ac2[0][t], e + 2 * k), pr(ac2[1][t], e + 2 * k), gk[t]); PIN(hv[k]); }
      }
      SCHED();
      fill_b(IC<POSTG[g][0]>{}); fill(IC<POSTG[g][1]>{}); fill(IC<POSTG[g][2]>{});
      if constexpr (g == 1) hv0 = hv;
      if constexpr (g == 3) { ld8(pv, poff, flg); st16<0>(l16, hv0, hb); }
      if constexpr ((g & 1) && g > 1) st16<((g >> 1) & 1) * 1024>(lt2[t], hv, hb);
    });
    FT(12); FT(13);
    FS(35); FT(14);
    waitv<W_V>();
    FT(15);
#pragma unroll
    for (int k = 0; k < 6; ++k) TOUCH(vst[k]);
    if (r < 2) {
#pragma unroll
      for (int k = 0; k < 6; ++k) vst[k] &= 4 * r - 8 + 6 * tp + k < 0 ? 0u : ~0u;
    }
#pragma unroll
    for (int k = 0; k < 6; ++k) *(LAS v4u*)(size_t)(vw + k * 8 * 272) = vst[k];
    const int tid = opq_tid();
    if (tid < 96) {
      const unsigned one = 4 * r - 8 + (tid >> 3) < 0 ? 0u : 0x3f803f80u;
      *(LAS v4u*)(size_t)(L0 + L_V + tid * 272 + 256) = (v4u){one, one, one, one};
    }
  }
  SCHED();
  FS(36);
  bar_l();
  FS(37);
  STAMP(5);
  f32x16 ga[2][2], sa[2];
  auto rdc = [&](auto I, auto U) {
    constexpr int i = I, u = U, st = i >> 2, ks = i & 3;
    if constexpr (ks == 0 && u == 0) {
      constexpr unsigned pk = CS[st][0] | (CS[st][1] << 8) | (CS[st][2] << 16) | (CS[st][3] << 24);
      unsigned k8 = 8 * kq; OPQ_S(k8);
      a0 = ca + ((pk >> k8) & 255u) * SLOT;
      a1 = a0 ^ 64u;
      TOUCH(a0); TOUCH(a1);
    }
    constexpr int d = 1024;
    if constexpr (u == 0) f[i & 1][0] = trfrag(a0 + ks * 2048, a0 + ks * 2048 + d);
    else f[i & 1][1] = trfrag(a1 + ks * 2048, a1 + ks * 2048 + d);
  };
  auto mmc = [&](auto I, auto M) {
    constexpr int i = I, m = M, u = m >> 1, t = m & 1;
    if constexpr (i == 0) mm_vva0(ga[u][t], pk[0][u], eh[t]);
    else if constexpr (i < 12) mm_vva(ga[u][t], pk[(i >> 2) % 3][2 * (i & 3) + u], eh[(2 * i + t) % 24]);
    else mm_vva(ga[u][t], f[i & 1][u], lh[(2 * i + t + 8) % 32]);
  };
  {
    auto vfrag = [&](int t, int sx) { return V4(trfrag(va + (t * 8 + 4 * sx) * 272, va + (t * 8 + 4 * sx + 2) * 272)); };
    bool dm[16];
    waitv<W_KQ>();
    TOUCH(pv);
    poll_finish(pv, poff, flg, tag);
    FS(38);
    f32x16 sb[2][2];
    float mx[2][2];
    auto mx16 = [&](int z, const f32x16& s) {
#pragma unroll
      for (int i = 0; i < 16; i += 4) { mx[z][0] = max3(mx[z][0], s[i], s[i + 1]); mx[z][1] = max3(mx[z][1], s[i + 2], s[i + 3]); }
      PIN(mx[z][0]); PIN(mx[z][1]);
    };
    {
      f32x16 sd[2], so[2];
      SCHED();
      mm_s0(sd[0], kf[8], qf[0]);
      SCHED();
      TOUCH(dl);
#pragma unroll
      for (int i = 0; i < 8; ++i) dm[i] = (i & 3) + 8 * (i >> 2) <= dl;
      SCHED();
      mm_s0(so[0], kf[0], qf[0]);
      SCHED();
      TOUCH(dl);
#pragma unroll
      for (int i = 8; i < 16; ++i) dm[i] = (i & 3) + 8 * (i >> 2) <= dl;
      SCHED();
      mm_s0(sd[1], kf[9], qf[1]);
      sfor<2>([&](auto Z) {
        constexpr int z = Z;
        SCHED();
        fill(IC<POSTM[z][0]>{}); fill(IC<POSTM[z][1]>{});
        if constexpr (z == 0) { nops<2>(); SCHED(); }
        float e[16];
#pragma unroll
        for (int i = 0; i < 8; ++i) { e[i] = dm[i] ? sd[z][i] : so[z][i]; PIN(e[i]); }
        SCHED();
        if constexpr (z == 0) mm_s0(so[1], kf[1], qf[1]); else mm_s0(sb[1][1], kf[2], qf[1]);
        SCHED();
#pragma unroll
        for (int i = 8; i < 16; ++i) { e[i] = dm[i] ? sd[z][i] : so[z][i]; PIN(e[i]); }
        SCHED();
        if constexpr (z == 0) mm_s0(sb[1][0], kf[1], qf[0]);
        SCHED();
        mx[z][0] = max3(e[0], e[1], e[2]); mx[z][1] = max3(e[3], e[4], e[5]);
        mx[z][0] = max3(mx[z][0], e[6], e[7]); mx[z][1] = max3(mx[z][1], e[8], e[9]); mx[z][0] = max3(mx[z][0], e[10], e[11]);
        mx[z][1] = max3(mx[z][1], e[12], e[13]); mx[z][0] = max3(mx[z][0], e[14], e[15]);
        PIN(mx[z][0]); PIN(mx[z][1]);
      });
      sfor<7>([&](auto TT) {
        constexpr int t = TT + 1;
        SCHED();
        if constexpr (t < 7) mm_s0(sb[(t + 1) & 1][0], kf[t + 1], qf[0]);
        SCHED();
        if constexpr (t == 4) { waitv<W_ST2>(); st8(FL2 * 128 + w * 8, tag, myf); }
        fill(IC<POSTM[t + 1][0]>{});
        mx16(0, sb[t & 1][0]);
        SCHED();
        if constexpr (t < 7) mm_s0(sb[(t + 1) & 1][1], kf[t + 2], qf[1]);
        SCHED();
        fill(IC<POSTM[t + 1][1]>{});
        mx16(1, sb[t & 1][1]);
      });
    }
    f32x16 nm[2], y[2];
#pragma unroll
    for (int z = 0; z < 2; ++z) {
      const unsigned u = __builtin_bit_cast(unsigned, __builtin_fmaxf(mx[z][0], mx[z][1]));
      const auto rr = __builtin_amdgcn_permlane32_swap(u, u, false, false);
      const unsigned r0 = rr[0], r1 = rr[1];
      const float m = __builtin_fmaxf(__builtin_bit_cast(float, r0), __builtin_bit_cast(float, r1));
#pragma unroll
      for (int i = 0; i < 16; ++i) nm[z][i] = -m;
      PIN(nm[z]);
    }
    FS(39);
    {
      f32x16 sd[2], so[2];
      SCHED();
      nops<2>();
      mm_sc(sd[0], kf[8], qf[0], nm[0]); mm_sc(so[0], kf[0], qf[0], nm[0]);
      sfor<2>([&](auto Z) {
        constexpr int z = Z;
        SCHED();
        const v4u v0 = vfrag(8 + z, 0), v1 = vfrag(8 + z, 1), u0 = vfrag(z, 0), u1 = vfrag(z, 1);
        SCHED();
        if constexpr (z == 0) { mm_sc(sd[1], kf[9], qf[1], nm[1]); nops<3>(); } else mm_sc(sb[1][0], kf[1], qf[0], nm[0]);
        f32x16 pd_, po_;
        auto q4 = [&](int o) {
#pragma unroll
          for (int i = o; i < o + 4; ++i) {
            const float p = __builtin_amdgcn_exp2f(dm[i] ? sd[z][i] : so[z][i]);
            pd_[i] = dm[i] ? p : 0.f;
            po_[i] = dm[i] ? 0.f : p;
            PIN(pd_[i]); PIN(po_[i]);
          }
        };
        SCHED();
        q4(0);
        fill(IC<POSTA[z][0]>{});
        SCHED();
        if constexpr (z == 0) mm_sc(so[1], kf[1], qf[1], nm[1]);
        q4(4);
        fill(IC<POSTA[z][1]>{});
        SCHED();
        const v4u pa = pack8(pd_, 0), pb = pack8(po_, 0);
        SCHED();
        mm_avv0(y[z], v0, pa);
        q4(8);
        fill(IC<POSTA[z][2]>{});
        SCHED();
        mm_avv_nn(y[z], u0, pb);
        q4(12);
        fill(IC<POSTA[z][3]>{});
        SCHED();
        const v4u pc = pack8(pd_, 8);
        PIN(pc);
        fill(IC<POSTA[z][4]>{});
        SCHED();
        const v4u pe = pack8(po_, 8);
        PIN(pe);
        SCHED();
        mm_avv_nn(y[z], v1, pc);
        fill(IC<POSTA[z][5]>{});
        nops<4>();
        mm_avv_nn(y[z], u1, pe);
      });
    }
    FS(40); FS(41);
    sfor<8>([&](auto KT) {
      constexpr int kt = KT + 1, st = kt + 1;
      constexpr bool useA = kt <= 7, useB = kt >= 2, ride = useA && useB;
      float p[16];
      v4u v0, v1;
      auto ex = [&](const f32x16& sv, int o, int n) {
#pragma unroll
        for (int i = o; i < o + n; ++i) { p[i] = __builtin_amdgcn_exp2f(sv[i]); PIN(p[i]); }
      };
      auto pk8 = [&](int o) { return (v4u){pk2(p[o], p[o + 1]), pk2(p[o + 2], p[o + 3]), pk2(p[o + 4], p[o + 5]), pk2(p[o + 6], p[o + 7])}; };
      auto half = [&](auto Z) {
        constexpr int z = Z, fb = ride ? 3 * z : 0;
        const f32x16& sv = sb[kt & 1][z];
        SCHED();
        if constexpr (st == 2) { park(IC<1>{}, IC<0>{}); park(IC<1>{}, IC<1>{}); }
        if constexpr (st == 3) park(IC<1>{}, IC<2 + z>{});
        if constexpr (st == 4 || st == 5) park(IC<2>{}, IC<(2 * st + z) & 3>{});
        ex(sv, 0, 8);
        const v4u pa = pk8(0);
        PIN(pa);
        SCHED();
        fill_a(IC<POSTA[st][fb]>{});
        ex(sv, 8, 2);
        SCHED();
        mm_avv_nn(y[z], v0, pa);
        SCHED();
        fill_b(IC<POSTA[st][fb]>{});
        ex(sv, 10, 6);
        const v4u pb = pk8(8);
        fill_a(IC<POSTA[st][fb + 1]>{});
        SCHED();
        mm_avv(y[z], v1, pb);
        SCHED();
        fill_b(IC<POSTA[st][fb + 1]>{});
        if constexpr (ride) fill(IC<POSTA[st][fb + 2]>{});
      };
      SCHED();
      if constexpr (st == 4) waitv<W_PK2>();
      if constexpr (st == 7) { waitv<W_P2>(); TOUCH(pv); poll_finish(pv, poff, flg, tag); FS(43); }
      v0 = vfrag(kt, 0); v1 = vfrag(kt, 1);
      SCHED();
      if constexpr (kt + 1 <= 8) mm_sc(sb[(kt + 1) & 1][1], kf[(kt + 1) % 10], qf[1], nm[1]);
      if constexpr (!useB) mm_sc(sb[(kt + 1) & 1][0], kf[(kt + 1) % 10], qf[0], nm[0]);
      if constexpr (useA) half(IC<0>{});
      SCHED();
      if constexpr (kt + 1 <= 7 && useB) mm_sc(sb[(kt + 1) & 1][0], kf[(kt + 1) % 10], qf[0], nm[0]);
      if constexpr (useB) half(IC<1>{});
      if constexpr (kt >= 6) { PIN(nm[0]); PIN(nm[1]); }
      if constexpr (st == 3) {
        poff = FL2 * 128 + 4 * kq * 128 + (pofw & ~1536u);
        ld8(pv, poff, flg);
        FS(42);
      }
    });
    SCHED();
    nops<12>();
    asm volatile("" : "+a"(y[0]), "+a"(y[1]));
    SCHED();
    st16<0>(kva + 8192, pack8(y[0], 0, __builtin_amdgcn_rcpf(y[0][8])), qkvh);
    st16<1024>(kva + 8192, pack8(y[1], 0, __builtin_amdgcn_rcpf(y[1][8])), qkvh);
  }
  STAMP(6);
  FS(44);
  FS(45);
  STAMP(7);
  v4u xr[2];
  SCHED();
  FS(46);
  waitbar<W_YA>();
  FS(47);
  fill_a(IC<POSTC[0][0]>{});
  sfor<28>([&](auto I) {
    constexpr int i = I;
    if constexpr ((i & 1) == 0 && i > 0) waitv<WL[i]>();
    sfor<4>([&](auto M) {
      constexpr int m = M;
      SCHED();
      mmc(IC<i>{}, M);
      SCHED();
      if constexpr (m == 0 && (i & 3) == 3 && i < 27) {
        constexpr int st = (i + 1) >> 2;
        FS(47 + st);
        if constexpr (st >= 3) waitv<WY[st % 7]>();
      }
      if constexpr (m == 0 && i == 3) { waitv<W_OST>(); st8(FL3 * 128 + w * 8, tag, myf); }
      if constexpr (m == 0 && i == 17) {
        poff = FL3 * 128 + pofw;
        ld8(pv, poff, flg);
      }
      if constexpr (m == 0 && i == 24) { FT(16); waitv<W_P3>(); TOUCH(pv); poll_finish(pv, poff, flg, tag); FT(17); }
      fill_b(IC<POSTC[i][m]>{});
      if constexpr ((m == 0 || m == 2) && i >= 11 && i < 27) rdc(IC<(i < 27 ? i + 1 : 0)>{}, IC<(m >> 1)>{});
      if constexpr (m < 3) fill_a(IC<POSTC[i][(m + 1) & 3]>{}); else fill_a(IC<POSTC[(i + 1) % 28][0] * (i < 27)>{});
    });
    if constexpr (i == 20) { ld16p(xr[0], xoff, px); ld16p(xr[1], xoff, px + 32); }
  });
  FS(54); FT(18);
  f32x4 gp[12];
  const unsigned rr = L0 + (kq < 2 ? 15 * SLOT : 10 * SLOT) + (kq & 1) * 12288 + l16;
  {
    static_assert(CS[7][1] == CS[7][0] + 1 && CS[7][3] == CS[7][0] + 3 && CS[8][1] == CS[8][0] + 1 && CS[8][3] == CS[8][0] + 3, "wo slots");
    const unsigned ob0 = ca + (l16 & 512u) + (kq >> 1 ? CS[8][0] : CS[7][0]) * SLOT, ob1 = ob0 ^ 64u;
    unsigned ad[3];
#pragma unroll
    for (int q = 0; q < 3; ++q) { const int d = (q + 1) ^ kq; ad[q] = L0 + (d < 2 ? 15 * SLOT : 10 * SLOT) + (d & 1) * 12288 + q * 4096 + l16; }
    auto rdo = [&](auto E, auto U) {
      constexpr int e = E, u = U;
      f[e & 1][u] = trfrag((u ? ob1 : ob0) + e * 2048, (u ? ob1 : ob0) + e * 2048 + 512);
    };
    SCHED();
    waitbar<W_G0>();
    FS(55); FT(19);
    TOUCH(xr[0]); TOUCH(xr[1]);
    rdo(IC<0>{}, IC<0>{}); rdo(IC<0>{}, IC<1>{});
    nops<6>();
    sfor<32>([&](auto G) {
      constexpr int g = G, e = g >> 1, u = g & 1;
      if constexpr ((g & 7) == 0) waitv<WO[g >> 3]>();
      SCHED();
      if constexpr (e == 0) mm_vva0(sa[u], f[0][u], of[0]); else mm_vva(sa[u], f[e & 1][u], of[e]);
      SCHED();
      if constexpr (g == 14) { FS(56); bar_l(); FS(57); }
      if constexpr (e < 15) rdo(IC<(e + 1) & 15>{}, IC<u>{});
      if constexpr (g < 12) {
        constexpr int q = g >> 2, k = g & 3;
        const f32x16& v = ga[(q + 1) >> 1][(q + 1) & 1];
        *(LAS f32x4*)(size_t)(ad[q % 3] + k * 1024) = (f32x4){v[4 * k], v[4 * k + 1], v[4 * k + 2], v[4 * k + 3]};
      }
      if constexpr (g >= 14 && g < 26) gp[g >= 14 && g < 26 ? ((g - 14) % 3) * 4 + (g - 14) / 3 : 0] = *(LAS f32x4*)(size_t)(rr + (((g - 14) % 3) * 4 + (g - 14) / 3) * 1024);
    });
  }
  STAMP(8);
  FS(58); FT(20);
  f32x2 gs[8];
  auto half2 = [](const f32x4& v, int k) { return k ? (f32x2){v[2], v[3]} : (f32x2){v[0], v[1]}; };
  auto silu4 = [&](int e0) {
#pragma unroll
    for (int e = e0; e < e0 + 4; ++e) {
      const f32x2 gv = ((pr(ga[0][0], 2 * e) + half2(gp[e >> 1], e & 1)) + half2(gp[4 + (e >> 1)], e & 1)) + half2(gp[8 + (e >> 1)], e & 1), z = gv * -1.44269504f;
      const f32x2 d = (f32x2){__builtin_amdgcn_exp2f(z.x), __builtin_amdgcn_exp2f(z.y)} + 1.0f;
      gs[e] = gv * (f32x2){__builtin_amdgcn_rcpf(d.x), __builtin_amdgcn_rcpf(d.y)};
      PIN(gs[e]);
    }
  };
  f32x4 sp[4];
  f32x2 xu[8];
  {
    const unsigned xw = L0 + (((kq ^ 2) & 2) ? 9 : 14) * SLOT + (kq & 1) * 4096 + l16, xrd = L0 + ((kq & 2) ? 9 : 14) * SLOT + (kq & 1) * 4096 + l16;
    silu4(0);
    SCHED();
#pragma unroll
    for (int k = 0; k < 4; ++k) *(LAS f32x4*)(size_t)(xw + k * 1024) = (f32x4){sa[1][4 * k], sa[1][4 * k + 1], sa[1][4 * k + 2], sa[1][4 * k + 3]};
    SCHED();
    silu4(4);
#pragma unroll
    for (int i = 0; i < 8; ++i) {
      const unsigned xi = xr[i >> 2][i & 3];
      xu[i] = (f32x2){__builtin_bit_cast(float, xi << 16), __builtin_bit_cast(float, xi & 0xffff0000u)};
      PIN(xu[i]);
    }
    SCHED();
    FS(59);
    bar_l();
    FS(60);
#pragma unroll
    for (int k = 0; k < 4; ++k) sp[k] = *(LAS f32x4*)(size_t)(xrd + k * 1024);
#pragma unroll
    for (int i = 0; i < 16; ++i) sa[0][i] += sp[i >> 2][i & 3];
  }
  FS(61); FS(62); FT(21); FT(22); FT(23); FT(24); FT(25);
  {
    SCHED();
#pragma unroll
    for (int t = 0; t < 2; ++t) {
      float yy[8];
#pragma unroll
      for (int i = 0; i < 4; ++i) {
        const f32x2 lo = pr(sa[0], 8 * t + 2 * (i >> 1)) * gs[4 * t + (i >> 1)], hi = pr(sa[0], 8 * t + 4 + 2 * (i >> 1)) * gs[4 * t + 2 + (i >> 1)];
        const float a = lo[i & 1], c = hi[i & 1];
        const auto sw = __builtin_amdgcn_permlane32_swap(__builtin_bit_cast(unsigned, a), __builtin_bit_cast(unsigned, c), false, false);
        const unsigned sw0 = sw[0], sw1 = sw[1];
        yy[i] = __builtin_bit_cast(float, sw0);
        yy[4 + i] = __builtin_bit_cast(float, sw1);
      }
      v4u o;
#pragma unroll
      for (int i = 0; i < 4; ++i) o[i] = pk2(xu[4 * t + i] + (f32x2){yy[2 * i], yy[2 * i + 1]});
      *(GAS v4u*)(pout + xoff + t * 32) = o;
    }
  }
  STAMP(9);
  FS(63);
  if constexpr (typeC) asm volatile(".Lfb_end1:"); else asm volatile(".Lfb_end0:");
}

__global__ void __launch_bounds__(256, 1)
fused_block(const char* __restrict__ xg, const char* __restrict__ wqkv, const char* __restrict__ wo, const char* __restrict__ wg,
            const char* __restrict__ wu, const char* __restrict__ wd, char* __restrict__ outg, char* __restrict__ wsp) {
  __shared__ __attribute__((aligned(1024))) char lds[LDS_BYTES];
  u64 px = (u64)xg, pq = (u64)wqkv, po = (u64)wo, pg = (u64)wg, pu = (u64)wu, pd = (u64)wd, pout = (u64)outg, ws = (u64)wsp;
  asm volatile("" : "+s"(px), "+s"(pq), "+s"(po), "+s"(pg), "+s"(pu), "+s"(pd), "+s"(pout), "+s"(ws));
  const unsigned L0 = (unsigned)(size_t)(LAS char*)lds;
  if ((blockIdx.x & 3) == 3) block_body<true>(px, pq, po, pg, pu, pd, pout, ws, L0);
  else block_body<false>(px, pq, po, pg, pu, pd, pout, ws, L0);
}

extern "C" void kernel_launch(void* const* d_in, const int* in_sizes, int n_in, void* d_out, int out_size, void* d_ws, size_t ws_size,
                              hipStream_t stream) {
  fused_block<<<256, 256, 0, stream>>>((const char*)d_in[0], (const char*)d_in[1], (const char*)d_in[2], (const char*)d_in[4],
                                       (const char*)d_in[5], (const char*)d_in[6], (char*)d_out, (char*)d_ws);
}
```
